```python
import torch

def _softmax(x, axis=-1):
    return torch.softmax(x, dim=axis)
def _gelu(x):
    return torch.nn.functional.gelu(x, approximate="tanh")
def _silu(x):
    return torch.nn.functional.silu(x)
def _sigmoid(x):
    return torch.sigmoid(x)
def _layernorm(x, axis=-1, eps=1e-5):
    m = x.mean(dim=axis, keepdim=True)
    v = x.var(dim=axis, keepdim=True, unbiased=False)
    return (x - m) / torch.sqrt(v + eps)
def _scatter_add(tgt, idx, src, axis=0):
    return tgt.clone().index_add_(axis, idx, src)
def _segment_sum(src, seg, n):
    out = torch.zeros((n,) + src.shape[1:], dtype=src.dtype)
    return out.index_add_(0, seg, src)
def _pad(x, pw):
    flat = tuple(v for pair in reversed(pw) for v in pair)
    return torch.nn.functional.pad(x, flat)

kernel_name = 'gated_mix__linear_kernel_hedgehog__sliding_local_dilated__prompt'

def setup_inputs():

    _w = (lambda a: (lambda t: t.to(torch.bfloat16) if t.is_floating_point() else t)(torch.as_tensor(a)))
    def _A_setup():
        B, S, D, H = 1, 2048, 640, 32
        Dh = D // H
        return {
            "x": _w((lambda *s: torch.randn(*s, dtype=torch.bfloat16) * 0.05)(B, S, D)),
            "wqkv": _w((lambda *s: torch.randn(*s, dtype=torch.bfloat16) * 1.6)(D, 3 * D)),
            "w_phi": _w((lambda *s: torch.randn(*s, dtype=torch.bfloat16) * 1.6)(H, Dh, Dh) * (Dh ** -0.5)),
            "wo": _w((lambda *s: torch.randn(*s, dtype=torch.bfloat16) * 0.1)(D, D)),
        }
    _A_in = _A_setup()

    def _B_setup():
        B, S, D = 1, 2048, 640
        return {
            "x": _w((lambda *s: torch.randn(*s, dtype=torch.bfloat16) * 0.05)(B, S, D)),
            "wqkv": _w((lambda *s: torch.randn(*s, dtype=torch.bfloat16) * 0.8)(D, 3 * D)),
            "wo": _w((lambda *s: torch.randn(*s, dtype=torch.bfloat16) * 0.05)(D, D)),
        }
    _B_in = _B_setup()

    return {
        "A_x": _A_in["x"],
        "A_wqkv": _A_in["wqkv"],
        "A_w_phi": _A_in["w_phi"],
        "A_wo": _A_in["wo"],
        "B_x": _B_in["x"],
        "B_wqkv": _B_in["wqkv"],
        "B_wo": _B_in["wo"],
        "gate_w": _w((lambda *s: torch.randn(*s, dtype=torch.bfloat16) * 0.05)(640) * 16.0),
    }

def reference(A_x, A_wqkv, A_w_phi, A_wo, B_x, B_wqkv, B_wo, gate_w):
    def _A_ref(x, wqkv, w_phi, wo):
        B, S, D = x.shape
        H = 32; Dh = D // H
        qkv = (lambda x, *d: x.permute(*d))((x @ wqkv).reshape(B, S, 3, H, Dh), 2, 0, 3, 1, 4)
        q, k, v = qkv[0], qkv[1], qkv[2]
        pq = torch.einsum("bhsd,hdf->bhsf", q, w_phi)
        pk = torch.einsum("bhsd,hdf->bhsf", k, w_phi)
        phi_q = torch.concatenate([_softmax(pq, axis=-1), _softmax(-pq, axis=-1)], axis=-1)
        phi_k = torch.concatenate([_softmax(pk, axis=-1), _softmax(-pk, axis=-1)], axis=-1)
        kv = torch.einsum("bhsf,bhsd->bhfd", phi_k, v)
        z  = torch.einsum("bhsf->bhf", phi_k)
        num = torch.einsum("bhqf,bhfd->bhqd", phi_q, kv)
        den = torch.einsum("bhqf,bhf->bhq", phi_q, z)[..., None] + 1e-6
        o = num / den
        return (lambda x, *d: x.permute(*d))(o, 0, 2, 1, 3).reshape(B, S, D) @ wo

    def _B_ref(x, wqkv, wo):
        B, S, D = x.shape
        H = 32; Dh = D // H
        W, dil = 8, 2
        qkv = (lambda x, *d: x.permute(*d))((x @ wqkv).reshape(B, S, 3, H, Dh), 2, 0, 3, 1, 4)
        q, k, v = qkv[0], qkv[1], qkv[2]
        scale = Dh ** -0.5
        pos = torch.arange(S)
        off = torch.arange(W) * dil
        idx = pos[:, None] - off[None, :]
        mask = idx >= 0
        idx_c = torch.where(mask, idx, 0).reshape(-1)
        kg = k[:, :, idx_c, :].reshape(B, H, S, W, Dh)
        vg = v[:, :, idx_c, :].reshape(B, H, S, W, Dh)
        logits = torch.einsum("bhsd,bhswd->bhsw", q, kg) * scale
        logits = torch.where(mask[None, None, :, :], logits, -1e9)
        attn = _softmax(logits, axis=-1)
        o = torch.einsum("bhsw,bhswd->bhsd", attn, vg)
        return (lambda x, *d: x.permute(*d))(o, 0, 2, 1, 3).reshape(B, S, D) @ wo

    x = A_x
    g = _sigmoid((x * gate_w).sum(-1, keepdims=True))
    a = _A_ref(x, A_wqkv, A_w_phi, A_wo)
    b = _B_ref(x, B_wqkv, B_wo)
    return g * a + (1 - g) * b
```

```cpp
#include <hip/hip_runtime.h>
#include <hip/hip_bf16.h>
#include <stdint.h>

typedef __bf16 bf16x8 __attribute__((ext_vector_type(8)));
typedef __bf16 bf16x4 __attribute__((ext_vector_type(4)));
typedef __bf16 bf16x2 __attribute__((ext_vector_type(2)));
typedef short s16x4 __attribute__((ext_vector_type(4)));
typedef float f32x2 __attribute__((ext_vector_type(2)));
typedef float f32x4 __attribute__((ext_vector_type(4)));
typedef float f32x16 __attribute__((ext_vector_type(16)));
typedef unsigned u32x2 __attribute__((ext_vector_type(2)));
typedef unsigned u32x4 __attribute__((ext_vector_type(4)));
#define LAS __attribute__((address_space(3)))
typedef LAS char* ldsp;

constexpr int S_ = 2048, D_ = 640, H_ = 32;
constexpr size_t WS_PHIQ = 0;
constexpr size_t WS_O    = WS_PHIQ + 32ull * 64 * 2560;
constexpr size_t WS_KVP  = WS_O + 2048ull * 1280 * 2;
constexpr size_t WS_G    = WS_KVP + 16ull * 32 * 840 * 4;
constexpr size_t WS_CNT  = WS_G + 2048 * 4;

constexpr int ST_X = 144 * 128, ST_W = 64 * 480, ST = ST_X + ST_W;
constexpr int K1_LDS = 3 * ST;
constexpr int CSTR = 488;
constexpr int PHSTR = 328;
constexpr int L_PHIK = 144 * CSTR;
constexpr int L_OST = 144 * CSTR;
constexpr int OSTR = 176;

#define SBAR() __builtin_amdgcn_sched_barrier(0)
#define WAIT_BAR(N) asm volatile("s_waitcnt vmcnt(" #N ") lgkmcnt(0)\n\ts_barrier" ::: "memory")
#define LBAR() asm volatile("s_waitcnt lgkmcnt(0)\n\ts_barrier" ::: "memory")
#define MFMA16(a, b, c) __builtin_amdgcn_mfma_f32_16x16x32_bf16(a, b, c, 0, 0, 0)
#define MFMA32(a, b, c) __builtin_amdgcn_mfma_f32_32x32x16_bf16(a, b, c, 0, 0, 0)

__device__ __forceinline__ void glds16(const char* sbase, unsigned voff, unsigned lds_dst) {
    unsigned sv;
    asm volatile("s_mov_b32 %0, m0\n\ts_mov_b32 m0, %3\n\ts_nop 0\n\tglobal_load_lds_dwordx4 %1, %2\n\ts_mov_b32 m0, %0"
                 : "=&s"(sv) : "v"(voff), "s"(sbase), "s"(lds_dst) : "memory");
}
__device__ __forceinline__ bf16x8 lds_b128(ldsp p) { return *(const LAS bf16x8*)p; }
__device__ __forceinline__ u32x2 lds_b64(ldsp p) { return *(const LAS u32x2*)p; }
__device__ __forceinline__ u32x2 lds_tr(ldsp p) {
    return __builtin_bit_cast(u32x2, __builtin_amdgcn_ds_read_tr16_b64_v4i16((LAS s16x4*)p));
}
__device__ __forceinline__ bf16x8 cat2(u32x2 a, u32x2 b) {
    u32x4 r = {a[0], a[1], b[0], b[1]};
    return __builtin_bit_cast(bf16x8, r);
}
__device__ __forceinline__ unsigned pk2(float lo, float hi) {
    bf16x2 r = {(__bf16)lo, (__bf16)hi};
    return __builtin_bit_cast(unsigned, r);
}
__device__ __forceinline__ float rbf(float x) { return (float)(__bf16)x; }
__device__ __forceinline__ float bflo(unsigned u) { return __uint_as_float(u << 16); }
__device__ __forceinline__ float bfhi(unsigned u) { return __uint_as_float(u & 0xffff0000u); }
__device__ __forceinline__ float dot2(unsigned a, unsigned b, float c) {
    return __builtin_amdgcn_fdot2_f32_bf16(__builtin_bit_cast(bf16x2, a), __builtin_bit_cast(bf16x2, b), c, false);
}
__device__ __forceinline__ float swap32(float v) {
    auto r = __builtin_amdgcn_permlane32_swap(__float_as_uint(v), __float_as_uint(v), false, false);
    return __uint_as_float((threadIdx.x & 32) ? r[0] : r[1]);
}
__device__ __forceinline__ int krow_inv(int rho) {
    return 8 * (2 * (rho >> 4) + ((rho >> 2) & 1)) + 4 * ((rho >> 3) & 1) + (rho & 3);
}

template <bool ISB, bool R16>
__device__ __forceinline__ void k1_main(ldsp lds, const char* gsrc, unsigned gstep, const unsigned (&goff)[6], unsigned ldsdst,
                                        int w, int lane, f32x4 (&acc)[4][4], f32x4 (&hacc)[2]) {
    const int wm = (w >> 1) & 1, side = w & 1;
    const int g = lane >> 4, l15 = lane & 15;
    const unsigned xsw = (unsigned)(l15 * 128 + ((g ^ (lane & 7)) * 16));
    const unsigned mb = (unsigned)((16 + 64 * wm) * 128);
    const unsigned xa01 = xsw + mb + ((!R16 && side) ? 4096u : 0u);
    const unsigned xa23 = xsw + mb + ((!R16 && side) ? 0u : 4096u);
    unsigned wa[4];
    {
        const unsigned base = (unsigned)(ST_X + (16 * (g >> 1) + 4 * (g & 1) + ((lane >> 2) & 3)) * 480 + (lane & 3) * 8);
#pragma unroll
        for (int i = 0; i < 4; ++i) {
            int n;
            if (R16) { int j = (i + 2 * wm) & 3; n = side ? 14 - j : j; }
            else { n = side ? 10 - i : 4 + i; }
            wa[i] = base + (unsigned)n * 32u;
        }
    }
    const unsigned ldsb = (unsigned)(uintptr_t)lds;
    auto issue = [&](int s, unsigned buf) {
        const char* sb = gsrc + (size_t)s * gstep;
#pragma unroll
        for (int i = 0; i < 6; ++i) glds16(sb, goff[i], ldsb + buf + ldsdst + i * 1024);
    };
    bf16x8 xf0[4], wf0[4], xh0, xf1[4], wf1[4], xh1;
#define RD(XF, WF, XH, BUF, HALF) do { \
        ldsp pb = lds + (BUF); \
        _Pragma("unroll") for (int i = 0; i < 4; ++i) \
            WF[i] = cat2(lds_tr(pb + wa[i] + (HALF) * 15360), lds_tr(pb + wa[i] + (HALF) * 15360 + 3840)); \
        XF[0] = lds_b128(pb + (xa01 ^ ((HALF) * 64))); XF[1] = lds_b128(pb + (xa01 ^ ((HALF) * 64)) + 2048); \
        XF[2] = lds_b128(pb + (xa23 ^ ((HALF) * 64))); XF[3] = lds_b128(pb + (xa23 ^ ((HALF) * 64)) + 2048); \
        if (ISB) XH = lds_b128(pb + (xsw ^ ((HALF) * 64))); \
    } while (0)
#define MM(XF, WF, XH) do { \
        _Pragma("unroll") for (int i = 0; i < 4; ++i) { \
            _Pragma("unroll") for (int a = 0; a < 4; ++a) { \
                if (R16 || i < 3 || a < 2) acc[i][a] = MFMA16(WF[i], XF[a], acc[i][a]); } \
            if (ISB && (i == 0 || (R16 && i == 1))) hacc[i] = MFMA16(WF[i], XH, hacc[i]); \
        } \
    } while (0)

    issue(0, 0); issue(1, ST); issue(2, 2 * ST);
    WAIT_BAR(12);
    unsigned cur = 0;
    RD(xf0, wf0, xh0, cur, 0);
#pragma unroll 1
    for (int s = 0; s < 9; ++s) {
        RD(xf1, wf1, xh1, cur, 1);
        SBAR();
        MM(xf0, wf0, xh0);
        SBAR();
        if (s < 8) WAIT_BAR(6); else WAIT_BAR(0);
        if (s < 7) issue(s + 3, cur);
        cur = (cur == 2 * ST) ? 0u : cur + ST;
        RD(xf0, wf0, xh0, cur, 0);
        SBAR();
        MM(xf1, wf1, xh1);
        SBAR();
    }
    RD(xf1, wf1, xh1, cur, 1);
    MM(xf0, wf0, xh0);
    MM(xf1, wf1, xh1);
#undef RD
#undef MM
}

template <bool ISB, bool R16>
__device__ __forceinline__ void k1_store_c(ldsp lds, int w, int lane, f32x4 (&acc)[4][4], f32x4 (&hacc)[2]) {
    const int wm = (w >> 1) & 1, side = w & 1;
    const int g = lane >> 4, l15 = lane & 15;
#pragma unroll
    for (int i = 0; i < 4; ++i) {
        int n;
        if (R16) { int j = (i + 2 * wm) & 3; n = side ? 14 - j : j; }
        else { n = side ? 10 - i : 4 + i; }
        const unsigned cb = (unsigned)(n * 32 + g * 8 + l15 * CSTR);
#pragma unroll
        for (int a = 0; a < 4; ++a) {
            if (R16 || i < 3 || a < 2) {
                const int mt = 4 * wm + ((!R16 && side) ? ((a + 2) & 3) : a);
                u32x2 v = {pk2(acc[i][a][0], acc[i][a][1]), pk2(acc[i][a][2], acc[i][a][3])};
                *(LAS u32x2*)(lds + cb + (unsigned)((16 + 16 * mt) * CSTR)) = v;
            }
        }
        if (ISB && (i == 0 || (R16 && i == 1))) {
            if (R16 || wm == 0) {
                u32x2 v = {pk2(hacc[i][0], hacc[i][1]), pk2(hacc[i][2], hacc[i][3])};
                *(LAS u32x2*)(lds + cb) = v;
            }
        }
    }
}

__global__ __launch_bounds__(512, 2) void k1_qkv(const __hip_bfloat16* __restrict__ x, const __hip_bfloat16* __restrict__ wA,
                                                   const __hip_bfloat16* __restrict__ wB, const __hip_bfloat16* __restrict__ wphi,
                                                   const __hip_bfloat16* __restrict__ gatew, char* __restrict__ ws) {
    __shared__ __attribute__((aligned(16))) char smem[K1_LDS];
    ldsp lds = (ldsp)smem;
    const int tid = threadIdx.x, lane = tid & 63;
    const int w = __builtin_amdgcn_readfirstlane(tid >> 6);
    const int b = blockIdx.x, xcd = b & 7, idx = b >> 3;
    const int rt = 8 * (xcd & 1) + (idx & 7);
    const int cg = 4 * (xcd >> 1) + (idx >> 3);
    const bool isB = cg >= 8;
    const int hg = cg & 7;
    const int r0 = rt * 128;

    u32x2 gx[3], gw[3];
    {
        const char* xr = (const char*)x + (size_t)(r0 + 8 * cg + w) * 1280;
#pragma unroll
        for (int c = 0; c < 3; ++c) {
            int ch = lane + 64 * c; ch = ch < 160 ? ch : 159;
            gx[c] = *(const u32x2*)(xr + ch * 8);
            gw[c] = *(const u32x2*)((const char*)gatew + ch * 8);
        }
    }
    unsigned short wp[12];
    const int r32 = lane & 31, hh = lane >> 5;
    {
        const int rho = (r32 & 3) + 4 * (r32 >> 3), ho = (r32 >> 2) & 1;
        const int f = rho < 10 ? 10 * ho + rho : 0;
        const unsigned short* wpp = (const unsigned short*)wphi + (size_t)(4 * hg + (w & 3)) * 400 + f;
#pragma unroll
        for (int j = 0; j < 8; ++j) wp[j] = wpp[(8 * hh + j) * 20];
#pragma unroll
        for (int j = 0; j < 4; ++j) wp[8 + j] = wpp[(16 + j) * 20];
    }

    unsigned goff[6];
    const char* gsrc; unsigned gstep; unsigned ldsdst;
    if (w < 3) {
#pragma unroll
        for (int i = 0; i < 6; ++i) {
            const int row = 8 * (6 * w + i) + (lane >> 3);
            int gr = r0 - 16 + row; gr = gr < 0 ? 0 : gr;
            goff[i] = (unsigned)(gr * 1280 + (((lane & 7) ^ (row & 7)) * 16));
        }
        gsrc = (const char*)x; gstep = 128; ldsdst = (unsigned)(6 * w * 1024);
    } else {
#pragma unroll
        for (int i = 0; i < 6; ++i) {
            const int c = 64 * (6 * (w - 3) + i) + lane;
            const int rho = c / 30, cc = c - rho * 30;
            const int seg = cc / 10, ch = cc - seg * 10;
            const int k = (rho & 32) + krow_inv(rho & 31);
            const int segcol = isB ? (seg == 0 ? 640 : (seg == 1 ? 0 : 1280)) : seg * 640;
            goff[i] = (unsigned)(k * 3840 + segcol * 2 + hg * 160 + ch * 16);
        }
        gsrc = (const char*)(isB ? wB : wA); gstep = 64 * 3840; ldsdst = (unsigned)(ST_X + 6 * (w - 3) * 1024);
    }

    f32x4 acc[4][4], hacc[2];
#pragma unroll
    for (int i = 0; i < 4; ++i)
#pragma unroll
        for (int a = 0; a < 4; ++a) acc[i][a] = f32x4{0.f, 0.f, 0.f, 0.f};
    hacc[0] = f32x4{0.f, 0.f, 0.f, 0.f}; hacc[1] = hacc[0];

    if (isB) {
        if (w < 4) { k1_main<true, true>(lds, gsrc, gstep, goff, ldsdst, w, lane, acc, hacc); LBAR(); k1_store_c<true, true>(lds, w, lane, acc, hacc); }
        else       { k1_main<true, false>(lds, gsrc, gstep, goff, ldsdst, w, lane, acc, hacc); LBAR(); k1_store_c<true, false>(lds, w, lane, acc, hacc); }
    } else {
        if (w < 4) { k1_main<false, true>(lds, gsrc, gstep, goff, ldsdst, w, lane, acc, hacc); LBAR(); k1_store_c<false, true>(lds, w, lane, acc, hacc); }
        else       { k1_main<false, false>(lds, gsrc, gstep, goff, ldsdst, w, lane, acc, hacc); LBAR(); k1_store_c<false, false>(lds, w, lane, acc, hacc); }
    }

    {
        float sum = 0.f;
#pragma unroll
        for (int c = 0; c < 3; ++c) {
            float p = rbf(bflo(gx[c][0]) * bflo(gw[c][0])) + rbf(bfhi(gx[c][0]) * bfhi(gw[c][0]))
                    + rbf(bflo(gx[c][1]) * bflo(gw[c][1])) + rbf(bfhi(gx[c][1]) * bfhi(gw[c][1]));
            if (c == 2 && lane >= 32) p = 0.f;
            sum += p;
        }
#pragma unroll
        for (int o = 32; o >= 1; o >>= 1) sum += __shfl_xor(sum, o);
        if (lane == 0) {
            const float z = rbf(sum);
            const float gv = rbf(1.f / (1.f + __expf(-z)));
            ((float*)(ws + WS_G))[r0 + 8 * cg + w] = gv;
        }
    }
    if (b == 0 && tid < 64) ((unsigned*)(ws + WS_CNT))[tid * 16] = 0u;
    LBAR();

    constexpr float LOG2E = 1.4426950408889634f;
    if (!isB) {
        const int hl = w & 3, which = w >> 2;
        const int h = 4 * hg + hl;
        bf16x8 wa0, wa1;
        {
            const int rho = (r32 & 3) + 4 * (r32 >> 3);
            const bool vr = rho < 10;
            u32x4 t0, t1;
#pragma unroll
            for (int j = 0; j < 4; ++j) t0[j] = vr ? ((unsigned)wp[2 * j] | ((unsigned)wp[2 * j + 1] << 16)) : 0u;
            const bool v1 = vr && hh == 0;
            t1[0] = v1 ? ((unsigned)wp[8] | ((unsigned)wp[9] << 16)) : 0u;
            t1[1] = v1 ? ((unsigned)wp[10] | ((unsigned)wp[11] << 16)) : 0u;
            t1[2] = 0u; t1[3] = 0u;
            wa0 = __builtin_bit_cast(bf16x8, t0); wa1 = __builtin_bit_cast(bf16x8, t1);
        }
#pragma unroll 1
        for (int rg = 0; rg < 4; ++rg) {
            ldsp cp = lds + (16 + 32 * rg + r32) * CSTR + (which * 80 + 20 * hl) * 2;
            bf16x8 b0 = cat2(lds_b64(cp + hh * 16), lds_b64(cp + hh * 16 + 8));
            u32x2 t = lds_b64(cp + 32);
            u32x4 t1 = {hh ? 0u : t[0], hh ? 0u : t[1], 0u, 0u};
            bf16x8 b1 = __builtin_bit_cast(bf16x8, t1);
            f32x16 p = {};
            p = MFMA32(wa0, b0, p);
            p = MFMA32(wa1, b1, p);
            float v[10];
#pragma unroll
            for (int i = 0; i < 10; ++i) v[i] = rbf(p[i]);
            float mx = v[0], mn = v[0];
#pragma unroll
            for (int i = 1; i < 10; ++i) { mx = fmaxf(mx, v[i]); mn = fminf(mn, v[i]); }
            mx = fmaxf(mx, swap32(mx)); mn = fminf(mn, swap32(mn));
            float ep[10], en[10], sp = 0.f, sn = 0.f;
            const float mxl = -mx * LOG2E, mnl = mn * LOG2E;
#pragma unroll
            for (int i = 0; i < 10; ++i) {
                ep[i] = __builtin_amdgcn_exp2f(fmaf(v[i], LOG2E, mxl));
                en[i] = __builtin_amdgcn_exp2f(fmaf(v[i], -LOG2E, mnl));
                sp += ep[i]; sn += en[i];
            }
            sp += swap32(sp); sn += swap32(sn);
            const float rp = 1.f / sp, rn = 1.f / sn;
            unsigned o[10];
#pragma unroll
            for (int i = 0; i < 5; ++i) {
                o[i] = pk2(ep[2 * i] * rp, ep[2 * i + 1] * rp);
                o[5 + i] = pk2(en[2 * i] * rn, en[2 * i + 1] * rn);
            }
            if (which == 0) {
                char* dst = ws + WS_PHIQ + ((size_t)h * 64 + (rt * 4 + rg)) * 2560;
                *(u32x4*)(dst + lane * 16) = u32x4{o[0], o[1], o[2], o[3]};
                *(u32x4*)(dst + 1024 + lane * 16) = u32x4{o[4], o[5], o[6], o[7]};
                *(u32x2*)(dst + 2048 + lane * 8) = u32x2{o[8], o[9]};
            } else {
                ldsp pp = lds + L_PHIK + (32 * rg + r32) * PHSTR + hl * 80 + hh * 40;
#pragma unroll
                for (int i = 0; i < 5; ++i) *(LAS u32x2*)(pp + 8 * i) = u32x2{o[2 * i], o[2 * i + 1]};
            }
        }
        LBAR();
        {
            const int nt = w >> 2;
            const int g = lane >> 4, q = (lane >> 2) & 3, pp = lane & 3, l15 = lane & 15;
            const int rowl = 4 * (4 * (g & 1) + q) + 2 * (g >> 1);
            ldsp pa = lds + L_PHIK + rowl * PHSTR + hl * 80 + pp * 8;
            ldsp pv = lds + (16 + rowl) * CSTR + (160 + 20 * hl + 16 * nt + 4 * pp) * 2;
            f32x4 kv[3];
#pragma unroll
            for (int m = 0; m < 3; ++m) kv[m] = f32x4{0.f, 0.f, 0.f, 0.f};
            const bool ones = (nt == 1) && (l15 == 4);
#pragma unroll
            for (int kk = 0; kk < 4; ++kk) {
                u32x2 v0 = lds_tr(pv + kk * 32 * CSTR), v1 = lds_tr(pv + kk * 32 * CSTR + CSTR);
                if (ones) { v0 = u32x2{0x3f803f80u, 0x3f803f80u}; v1 = v0; }
                bf16x8 vb = cat2(v0, v1);
#pragma unroll
                for (int m = 0; m < 3; ++m) {
                    bf16x8 af = cat2(lds_tr(pa + kk * 32 * PHSTR + m * 32), lds_tr(pa + kk * 32 * PHSTR + PHSTR + m * 32));
                    kv[m] = MFMA16(af, vb, kv[m]);
                }
            }
            const int d = 16 * nt + l15;
            float* dst = (float*)(ws + WS_KVP) + ((size_t)rt * 32 + h) * 840 + d * 40 + 4 * g;
            if (d < 21) {
                *(f32x4*)(dst) = kv[0];
                *(f32x4*)(dst + 16) = kv[1];
                if (g < 2) *(f32x4*)(dst + 32) = kv[2];
            }
        }
    } else {
        const int hl = w & 3;
        const int row = 64 * (w >> 2) + lane;
        const int srow = r0 + row;
        ldsp base = lds + (16 + row) * CSTR + 40 * hl;
        unsigned qv[10];
#pragma unroll
        for (int i = 0; i < 5; ++i) { u32x2 t = lds_b64(base + 160 + 8 * i); qv[2 * i] = t[0]; qv[2 * i + 1] = t[1]; }
        float lg[8];
        const float scale = 0.22360679774997896f;
#pragma unroll
        for (int j = 0; j < 8; ++j) {
            ldsp kp = base - 2 * j * CSTR;
            float a = 0.f;
#pragma unroll
            for (int i = 0; i < 5; ++i) { u32x2 t = lds_b64(kp + 8 * i); a = dot2(qv[2 * i], t[0], a); a = dot2(qv[2 * i + 1], t[1], a); }
            a = rbf(rbf(a) * scale);
            lg[j] = (srow - 2 * j >= 0) ? a : -1e9f;
        }
        float mx = lg[0];
#pragma unroll
        for (int j = 1; j < 8; ++j) mx = fmaxf(mx, lg[j]);
        float sum = 0.f;
#pragma unroll
        for (int j = 0; j < 8; ++j) { lg[j] = __builtin_amdgcn_exp2f((lg[j] - mx) * LOG2E); sum += lg[j]; }
        const float rs = 1.f / sum;
        float o[20];
#pragma unroll
        for (int i = 0; i < 20; ++i) o[i] = 0.f;
#pragma unroll
        for (int jp = 0; jp < 4; ++jp) {
            const unsigned pw = pk2(lg[2 * jp] * rs, lg[2 * jp + 1] * rs);
            ldsp va = base + 320 - 4 * jp * CSTR;
            ldsp vb = va - 2 * CSTR;
#pragma unroll
            for (int i = 0; i < 5; ++i) {
                u32x2 ta = lds_b64(va + 8 * i), tb = lds_b64(vb + 8 * i);
#pragma unroll
                for (int e = 0; e < 2; ++e) {
                    const unsigned lo = __builtin_amdgcn_perm(tb[e], ta[e], 0x05040100u);
                    const unsigned hi = __builtin_amdgcn_perm(tb[e], ta[e], 0x07060302u);
                    o[4 * i + 2 * e] = dot2(pw, lo, o[4 * i + 2 * e]);
                    o[4 * i + 2 * e + 1] = dot2(pw, hi, o[4 * i + 2 * e + 1]);
                }
            }
        }
        ldsp op = lds + L_OST + row * OSTR + 40 * hl;
#pragma unroll
        for (int i = 0; i < 5; ++i) *(LAS u32x2*)(op + 8 * i) = u32x2{pk2(o[4 * i], o[4 * i + 1]), pk2(o[4 * i + 2], o[4 * i + 3])};
        LBAR();
#pragma unroll
        for (int it = 0; it < 3; ++it) {
            const int c = tid + 512 * it;
            if (c < 1280) {
                const int rr = c / 10, ch = c - rr * 10;
                u32x4 v = *(const LAS u32x4*)(lds + L_OST + rr * OSTR + ch * 16);
                *(u32x4*)(ws + WS_O + (size_t)(r0 + rr) * 2560 + hg * 160 + ch * 16) = v;
            }
        }
    }
}

__global__ __launch_bounds__(512, 2) void k2_oa(char* __restrict__ ws) {
    __shared__ __attribute__((aligned(16))) char smem[2048];
    ldsp lds = (ldsp)smem;
    const int tid = threadIdx.x, lane = tid & 63;
    const int w = __builtin_amdgcn_readfirstlane(tid >> 6);
    const int h = blockIdx.x & 31, c = blockIdx.x >> 5;
    const int rgp = c * 8 + w;
    const char* pq = ws + WS_PHIQ + ((size_t)h * 64 + rgp) * 2560;
    const u32x4 q0 = *(const u32x4*)(pq + lane * 16);
    const u32x4 q1 = *(const u32x4*)(pq + 1024 + lane * 16);
    const u32x2 q2 = *(const u32x2*)(pq + 2048 + lane * 8);
    {
        const float* kp = (const float*)(ws + WS_KVP) + (size_t)h * 840;
        const int e1 = tid + 512 < 840 ? tid + 512 : 839;
        float s0 = 0.f, s1 = 0.f;
#pragma unroll
        for (int r = 0; r < 16; ++r) { s0 += kp[(size_t)r * 32 * 840 + tid]; s1 += kp[(size_t)r * 32 * 840 + e1]; }
        *(LAS __bf16*)(lds + tid * 2) = (__bf16)s0;
        if (tid + 512 < 840) *(LAS __bf16*)(lds + (tid + 512) * 2) = (__bf16)s1;
    }
    __syncthreads();
    const int r32 = lane & 31, hh = lane >> 5;
    const int rho = (r32 & 3) + 4 * (r32 >> 3), ho = (r32 >> 2) & 1;
    const bool vr = rho <= 10;
    const int d = rho < 10 ? 10 * ho + rho : 20;
    ldsp ap = lds + d * 80 + hh * 40;
    u32x2 a00 = lds_b64(ap), a01 = lds_b64(ap + 8), a10 = lds_b64(ap + 16), a11 = lds_b64(ap + 24), a20 = lds_b64(ap + 32);
    const u32x2 z2 = {0u, 0u};
    if (!vr) { a00 = z2; a01 = z2; a10 = z2; a11 = z2; a20 = z2; }
    f32x16 p = {};
    p = MFMA32(cat2(a00, a01), __builtin_bit_cast(bf16x8, q0), p);
    p = MFMA32(cat2(a10, a11), __builtin_bit_cast(bf16x8, q1), p);
    p = MFMA32(cat2(a20, z2), cat2(q2, z2), p);
    const float den = rbf(rbf(p[10]) + 1e-6f);
    const float rd = 1.f / den;
    unsigned o[5];
#pragma unroll
    for (int i = 0; i < 5; ++i) o[i] = pk2(rbf(p[2 * i]) * rd, rbf(p[2 * i + 1]) * rd);
    unsigned* dst = (unsigned*)(ws + WS_O + (size_t)(rgp * 32 + r32) * 2560 + 1280 + h * 40 + hh * 20);
#pragma unroll
    for (int i = 0; i < 5; ++i) dst[i] = o[i];
}

constexpr int S3_X = 64 * 256, S3_W = 128 * 160, S3 = S3_X + S3_W;
constexpr int K3_LDS = 4 * S3;
__global__ __launch_bounds__(512, 2) void k3_out(const __hip_bfloat16* __restrict__ woA, const __hip_bfloat16* __restrict__ woB,
                                                   const char* __restrict__ ws, __hip_bfloat16* __restrict__ out) {
    __shared__ __attribute__((aligned(16))) char smem[K3_LDS];
    ldsp lds = (ldsp)smem;
    const int tid = threadIdx.x, lane = tid & 63;
    const int w = __builtin_amdgcn_readfirstlane(tid >> 6);
    const int b = blockIdx.x, xcd = b & 7, idx = b >> 3;
    const int rt = 8 * (xcd & 3) + (idx & 7);
    const int ct = 4 * (xcd >> 2) + (idx >> 3);
    const int r0 = rt * 64, n0 = ct * 80;
    const int wm = w & 1, kw = w >> 1;
    const int g = lane >> 4, l15 = lane & 15;
    float gv[2];
    gv[0] = ((const float*)(ws + WS_G))[r0 + 32 * wm + l15];
    gv[1] = ((const float*)(ws + WS_G))[r0 + 32 * wm + 16 + l15];

    unsigned goff[5];
    unsigned ldsdst;
    if (w < 4) {
#pragma unroll
        for (int i = 0; i < 4; ++i) {
            const int row = 4 * (4 * w + i) + (lane >> 4);
            goff[i] = (unsigned)((r0 + row) * 2560 + (((lane & 15) ^ (row & 15)) * 16));
        }
        goff[4] = 0; ldsdst = (unsigned)(4 * w * 1024);
    } else {
#pragma unroll
        for (int i = 0; i < 5; ++i) {
            const int c = 64 * (5 * (w - 4) + i) + lane;
            const int rho = c / 10, ch = c - rho * 10;
            const int k = (rho & ~31) + krow_inv(rho & 31);
            goff[i] = (unsigned)(k * 1280 + n0 * 2 + ch * 16);
        }
        ldsdst = (unsigned)(S3_X + 5 * (w - 4) * 1024);
    }
    const unsigned ldsb = (unsigned)(uintptr_t)lds;
    auto issue = [&](int st, unsigned buf) {
        if (w < 4) {
            const char* sb = ws + WS_O + st * 256;
#pragma unroll
            for (int i = 0; i < 4; ++i) glds16(sb, goff[i], ldsb + buf + ldsdst + i * 1024);
        } else {
            const char* sb = (st < 5) ? (const char*)woB + (size_t)st * 128 * 1280 : (const char*)woA + (size_t)(st - 5) * 128 * 1280;
#pragma unroll
            for (int i = 0; i < 5; ++i) glds16(sb, goff[i], ldsb + buf + ldsdst + i * 1024);
        }
    };
    const unsigned xa = (unsigned)((32 * wm + l15) * 256 + (((4 * kw + g) ^ l15) * 16));
    const unsigned wa = (unsigned)(S3_X + (32 * kw + 16 * (g >> 1) + 4 * (g & 1) + ((lane >> 2) & 3)) * 160 + (lane & 3) * 8);
    f32x4 accb[5][2], acca[5][2];
#pragma unroll
    for (int n = 0; n < 5; ++n) { accb[n][0] = f32x4{0.f, 0.f, 0.f, 0.f}; accb[n][1] = accb[n][0]; acca[n][0] = accb[n][0]; acca[n][1] = accb[n][0]; }

    issue(0, 0); issue(1, S3); issue(2, 2 * S3);
#define STEP(ACC, ST_) do { \
        if ((ST_) < 8) { if (w < 4) WAIT_BAR(8); else WAIT_BAR(10); } \
        else if ((ST_) == 8) { if (w < 4) WAIT_BAR(4); else WAIT_BAR(5); } \
        else WAIT_BAR(0); \
        if ((ST_) < 7) issue((ST_) + 3, (unsigned)((((ST_) + 3) & 3) * S3)); \
        ldsp pb = lds + ((ST_) & 3) * S3; \
        bf16x8 x0 = lds_b128(pb + xa), x1 = lds_b128(pb + xa + 4096); \
        _Pragma("unroll") for (int n = 0; n < 5; ++n) { \
            bf16x8 wf = cat2(lds_tr(pb + wa + n * 32), lds_tr(pb + wa + n * 32 + 1280)); \
            ACC[n][0] = MFMA16(wf, x0, ACC[n][0]); ACC[n][1] = MFMA16(wf, x1, ACC[n][1]); } \
    } while (0)
#pragma unroll 1
    for (int st = 0; st < 5; ++st) STEP(accb, st);
#pragma unroll 1
    for (int st = 5; st < 10; ++st) STEP(acca, st);
#undef STEP
    LBAR();
    {
#pragma unroll
        for (int n = 0; n < 5; ++n)
#pragma unroll
            for (int m = 0; m < 2; ++m) {
                const float ga = gv[m], gb = rbf(1.f - gv[m]);
                f32x4 y;
#pragma unroll
                for (int e = 0; e < 4; ++e) y[e] = ga * acca[n][m][e] + gb * accb[n][m][e];
                *(LAS f32x4*)(lds + (kw * 64 + 32 * wm + 16 * m + l15) * 336 + (16 * n + 4 * g) * 4) = y;
            }
    }
    LBAR();
#pragma unroll
    for (int it = 0; it < 2; ++it) {
        const int c = tid + 512 * it;
        if (c < 640) {
            const int rr = c / 10, ch = c - rr * 10;
            f32x4 s0 = {0.f, 0.f, 0.f, 0.f}, s1 = s0;
#pragma unroll
            for (int k = 0; k < 4; ++k) {
                ldsp p = lds + (k * 64 + rr) * 336 + ch * 32;
                s0 += *(const LAS f32x4*)p; s1 += *(const LAS f32x4*)(p + 16);
            }
            u32x4 v = {pk2(s0[0], s0[1]), pk2(s0[2], s0[3]), pk2(s1[0], s1[1]), pk2(s1[2], s1[3])};
            *(u32x4*)((char*)out + (size_t)(r0 + rr) * 1280 + n0 * 2 + ch * 16) = v;
        }
    }
}

extern "C" void kernel_launch(void* const* d_in, const int* in_sizes, int n_in,
                              void* d_out, int out_size, void* d_ws, size_t ws_size,
                              hipStream_t stream) {
    const __hip_bfloat16* x    = (const __hip_bfloat16*)d_in[0];
    const __hip_bfloat16* wA   = (const __hip_bfloat16*)d_in[1];
    const __hip_bfloat16* wphi = (const __hip_bfloat16*)d_in[2];
    const __hip_bfloat16* woA  = (const __hip_bfloat16*)d_in[3];
    const __hip_bfloat16* wB   = (const __hip_bfloat16*)d_in[5];
    const __hip_bfloat16* woB  = (const __hip_bfloat16*)d_in[6];
    const __hip_bfloat16* gw   = (const __hip_bfloat16*)d_in[7];
    char* ws = (char*)d_ws;
    k1_qkv<<<256, 512, 0, stream>>>(x, wA, wB, wphi, gw, ws);
    k2_oa<<<256, 512, 0, stream>>>(ws);
    k3_out<<<256, 512, 0, stream>>>(woA, woB, ws, (__hip_bfloat16*)d_out);
}
```

```cpp
#include <hip/hip_runtime.h>
#include <hip/hip_bf16.h>
#include <stdint.h>

typedef __bf16 bf16x8 __attribute__((ext_vector_type(8)));
typedef __bf16 bf16x2 __attribute__((ext_vector_type(2)));
typedef short s16x4 __attribute__((ext_vector_type(4)));
typedef float f32x2 __attribute__((ext_vector_type(2)));
typedef float f32x4 __attribute__((ext_vector_type(4)));
typedef float f32x16 __attribute__((ext_vector_type(16)));
typedef unsigned u32x2 __attribute__((ext_vector_type(2)));
typedef unsigned u32x4 __attribute__((ext_vector_type(4)));
#define LAS __attribute__((address_space(3)))
typedef LAS char* ldsp;

constexpr size_t WS_QF  = 0;
constexpr size_t WS_O   = WS_QF + 32ull * 64 * 1280;
constexpr size_t WS_KVP = WS_O + 32ull * 8 * 20480;
constexpr size_t WS_WOP = WS_KVP + 16ull * 32 * 840 * 4;
constexpr size_t WS_WPF = WS_WOP + 64ull * 25600;
constexpr size_t WS_G   = WS_WPF + 32ull * 64 * 32;
constexpr size_t WS_CNT = WS_G + 2048 * 4;

constexpr int ST_X = 144 * 128, ST_W = 3 * 64 * 160, ST = ST_X + ST_W;
constexpr int K1_LDS = 3 * ST;
constexpr int CSTR = 488;
constexpr int PHSTR = 328;
constexpr int L_PHIK = 144 * CSTR;
constexpr int L_GW = K1_LDS;
constexpr int L_WPH = L_GW + 2048;
constexpr int L_WO = L_WPH + 4096;
constexpr int K1_LDS_ALL = L_WO + 8192;

#define STAMP(i)
#define STAMP_DECL
#define STAMP_OUT(kid)
#define STAMP_ARG
#define STAMP_PASS

#define SBAR() __builtin_amdgcn_sched_barrier(0)
#define VBAR(N) asm volatile("s_waitcnt vmcnt(%0)\n\ts_barrier" :: "i"(N) : "memory")
#define LBAR() asm volatile("s_waitcnt lgkmcnt(0)\n\ts_barrier" ::: "memory")
#define MFMA16(a, b, c) __builtin_amdgcn_mfma_f32_16x16x32_bf16(a, b, c, 0, 0, 0)
#define MFMA32(a, b, c) __builtin_amdgcn_mfma_f32_32x32x16_bf16(a, b, c, 0, 0, 0)

__device__ __forceinline__ void glds16(const char* sbase, unsigned voff, unsigned lds_dst) {
    unsigned sv;
    asm volatile("s_mov_b32 %0, m0\n\ts_mov_b32 m0, %3\n\ts_nop 0\n\tglobal_load_lds_dwordx4 %1, %2\n\ts_mov_b32 m0, %0"
                 : "=&s"(sv) : "v"(voff), "s"(sbase), "s"(lds_dst) : "memory");
}
__device__ __forceinline__ void glds16_sc1(const char* sbase, unsigned voff, unsigned lds_dst) {
    unsigned sv;
    asm volatile("s_mov_b32 %0, m0\n\ts_mov_b32 m0, %3\n\ts_nop 0\n\tglobal_load_lds_dwordx4 %1, %2 sc1\n\ts_mov_b32 m0, %0"
                 : "=&s"(sv) : "v"(voff), "s"(sbase), "s"(lds_dst) : "memory");
}
#define STAG 1
#define WPF 1
#define OSC1 4
#define K1AUX 16
#define WSRSRC(ws) __builtin_amdgcn_make_buffer_rsrc((void*)(ws), 0, (int)(WS_CNT + 4096), 0x00020000)
#define ST128(rs, off, v) __builtin_amdgcn_raw_buffer_store_b128((v), rs, (unsigned)(off), 0, K1AUX)
#define ST64(rs, off, v) __builtin_amdgcn_raw_buffer_store_b64((v), rs, (unsigned)(off), 0, K1AUX)
#define ST32(rs, off, v) __builtin_amdgcn_raw_buffer_store_b32((v), rs, (unsigned)(off), 0, K1AUX)
template <int N, bool SC1>
__device__ __forceinline__ void gcopy(const char* sbase, unsigned voff, unsigned lds_dst) {
    unsigned sv;
#define GL_(o) "global_load_lds_dwordx4 %1, %2 offset:" #o
#define GC_(body) asm volatile("s_mov_b32 %0, m0\n\ts_mov_b32 m0, %3\n\ts_nop 0\n\t" body "s_mov_b32 m0, %0" : "=&s"(sv) : "v"(voff), "s"(sbase), "s"(lds_dst) : "memory")
    if constexpr (SC1) {
        if constexpr (N == 4) GC_(GL_(0) " sc1\n\t" GL_(1024) " sc1\n\t" GL_(2048) " sc1\n\t" GL_(3072) " sc1\n\t");
        else if constexpr (N == 2) GC_(GL_(0) " sc1\n\t" GL_(1024) " sc1\n\t");
        else GC_(GL_(0) " sc1\n\t");
    } else {
        if constexpr (N == 4) GC_(GL_(0) "\n\t" GL_(1024) "\n\t" GL_(2048) "\n\t" GL_(3072) "\n\t");
        else if constexpr (N == 2) GC_(GL_(0) "\n\t" GL_(1024) "\n\t");
        else GC_(GL_(0) "\n\t");
    }
#undef GL_
#undef GC_
}
__device__ __forceinline__ bf16x8 lds_b128(ldsp p) { return *(const LAS bf16x8*)p; }
__device__ __forceinline__ u32x2 lds_b64(ldsp p) { return *(const LAS u32x2*)p; }
__device__ __forceinline__ u32x2 lds_tr(ldsp p) {
    return __builtin_bit_cast(u32x2, __builtin_amdgcn_ds_read_tr16_b64_v4i16((LAS s16x4*)p));
}
__device__ __forceinline__ bf16x8 cat2(u32x2 a, u32x2 b) {
    u32x4 r = {a[0], a[1], b[0], b[1]};
    return __builtin_bit_cast(bf16x8, r);
}
__device__ __forceinline__ unsigned pk2(float lo, float hi) {
    bf16x2 r = {(__bf16)lo, (__bf16)hi};
    return __builtin_bit_cast(unsigned, r);
}
__device__ __forceinline__ float rbf(float x) { return (float)(__bf16)x; }
__device__ __forceinline__ float bflo(unsigned u) { return __uint_as_float(u << 16); }
__device__ __forceinline__ float bfhi(unsigned u) { return __uint_as_float(u & 0xffff0000u); }
__device__ __forceinline__ float dot2(unsigned a, unsigned b, float c) {
    return __builtin_amdgcn_fdot2_f32_bf16(__builtin_bit_cast(bf16x2, a), __builtin_bit_cast(bf16x2, b), c, false);
}
#define XHALF(v, A, B) do { auto r_ = __builtin_amdgcn_permlane32_swap(__float_as_uint(v), __float_as_uint(v), false, false); A = __uint_as_float(r_[0]); B = __uint_as_float(r_[1]); } while (0)
__device__ __forceinline__ int krow_inv(int rho) {
    return (rho & 19) | ((rho & 4) << 1) | ((rho & 8) >> 1);
}
constexpr float LOG2E = 1.4426950408889634f;

__device__ __forceinline__ void feat_map(const f32x16& p, unsigned (&o)[10]) {
    float mx = fmaxf(fmaxf(p[0], p[1]), p[2]), mn = fminf(fminf(p[0], p[1]), p[2]);
    mx = fmaxf(fmaxf(mx, p[3]), p[4]); mn = fminf(fminf(mn, p[3]), p[4]);
    mx = fmaxf(fmaxf(mx, p[5]), p[6]); mn = fminf(fminf(mn, p[5]), p[6]);
    mx = fmaxf(fmaxf(mx, p[7]), p[8]); mn = fminf(fminf(mn, p[7]), p[8]);
    mx = fmaxf(mx, p[9]); mn = fminf(mn, p[9]);
    float a, b;
    XHALF(mx, a, b); mx = fmaxf(a, b);
    XHALF(mn, a, b); mn = fminf(a, b);
    const float mxl = -mx * LOG2E, mnl = mn * LOG2E;
    float ep[10], en[10];
#pragma unroll
    for (int i = 0; i < 10; ++i) {
        ep[i] = __builtin_amdgcn_exp2f(fmaf(p[i], LOG2E, mxl));
        en[i] = __builtin_amdgcn_exp2f(fmaf(p[i], -LOG2E, mnl));
    }
    float sp = ((ep[0] + ep[1]) + (ep[2] + ep[3])) + ((ep[4] + ep[5]) + (ep[6] + ep[7])) + (ep[8] + ep[9]);
    float sn = ((en[0] + en[1]) + (en[2] + en[3])) + ((en[4] + en[5]) + (en[6] + en[7])) + (en[8] + en[9]);
    XHALF(sp, a, b); sp = a + b;
    XHALF(sn, a, b); sn = a + b;
    const float rp = __builtin_amdgcn_rcpf(sp), rn = __builtin_amdgcn_rcpf(sn);
#pragma unroll
    for (int i = 0; i < 5; ++i) {
        o[i] = pk2(ep[2 * i] * rp, ep[2 * i + 1] * rp);
        o[5 + i] = pk2(en[2 * i] * rn, en[2 * i + 1] * rn);
    }
}

template <int CW> struct K1Map {
    static constexpr int WM = CW >> 1, SIDE = CW & 1;
    static constexpr int nloc(int i) { return (WM == 1 && i < 5) ? (i + 3) % 5 : i; }
    static constexpr int ntile(int i) { return SIDE ? 14 - nloc(i) : nloc(i); }
    static constexpr int mtile(int a) { return 4 * WM + (SIDE ? ((a + 2) & 3) : a); }
    static constexpr int woff(int i) { return ST_X + (ntile(i) / 5) * 10240 + (ntile(i) % 5) * 32; }
};

template <int CW, bool ISB>
__device__ __forceinline__ void k1_compute(ldsp lds, int lane, int koff, float* gout STAMP_ARG) {
    using M = K1Map<CW>;
    constexpr bool GATE = !ISB && (CW & 1) == 0;
    const int g = lane >> 4, l15 = lane & 15;
    const unsigned xs0 = (unsigned)(l15 * 128 + ((g ^ (lane & 7)) * 16));
    const unsigned wb = (unsigned)((16 * (g >> 1) + 4 * (g & 1) + ((lane >> 2) & 3)) * 160 + (lane & 3) * 8);
    f32x4 acc[8][4], hacc[4];
#pragma unroll
    for (int i = 0; i < 8; ++i)
#pragma unroll
        for (int a = 0; a < 4; ++a) acc[i][a] = f32x4{0.f, 0.f, 0.f, 0.f};
#pragma unroll
    for (int i = 0; i < 4; ++i) hacc[i] = f32x4{0.f, 0.f, 0.f, 0.f};
    unsigned gk = (unsigned)(L_GW + koff * 128 + g * 16);
    bf16x8 xf0[4], wf0[8], xh0, xf1[4], wf1[8], xh1;
#define RD(XF, WF, XH, BUF, HALF) do { \
        ldsp px = lds + (BUF) + (xs0 ^ ((HALF) * 64)); ldsp pw = lds + (BUF) + wb; \
        _Pragma("unroll") for (int a = 0; a < 4; ++a) XF[a] = lds_b128(px + (16 + 16 * M::mtile(a)) * 128); \
        if (ISB) XH = lds_b128(px); \
        if (GATE) XH = lds_b128(lds + gk + (HALF) * 64); \
        _Pragma("unroll") for (int i = 0; i < 8; ++i) \
            WF[i] = cat2(lds_tr(pw + M::woff(i) + (HALF) * 5120), lds_tr(pw + M::woff(i) + (HALF) * 5120 + 1280)); \
    } while (0)
#define MM(XF, WF, XH) do { \
        _Pragma("unroll") for (int i = 0; i < 8; ++i) { \
            _Pragma("unroll") for (int a = 0; a < 4; ++a) { \
                if (i < 7 || a < 2) acc[i][a] = MFMA16(WF[i], XF[a], acc[i][a]); } \
            if (ISB && i < 3) hacc[i] = MFMA16(WF[i], XH, hacc[i]); \
            if (GATE && i < 4) hacc[i] = MFMA16(XH, XF[i], hacc[i]); \
        } \
    } while (0)
#define ILV() do { \
        _Pragma("unroll") for (int t_ = 0; t_ < ((ISB || GATE) ? 21 : 20); ++t_) { \
            __builtin_amdgcn_sched_group_barrier(0x008, 1, 0); __builtin_amdgcn_sched_group_barrier(0x100, 1, 0); } \
        __builtin_amdgcn_sched_group_barrier(0x008, 13, 0); \
    } while (0)
    LBAR();
    STAMP(2);
    unsigned cur = 0;
    RD(xf0, wf0, xh0, cur, 0);
    SBAR();
#pragma unroll 1
    for (int s = 0; s < 9; ++s) {
        RD(xf1, wf1, xh1, cur, 1);
        MM(xf0, wf0, xh0);
        ILV();
        SBAR();
        LBAR();
        cur = (cur == 2 * ST) ? 0u : cur + ST;
        if (GATE) { gk += 128; if (gk >= (unsigned)(L_GW + 1280)) gk -= 1280; }
        RD(xf0, wf0, xh0, cur, 0);
        MM(xf1, wf1, xh1);
        ILV();
        SBAR();
    }
    RD(xf1, wf1, xh1, cur, 1);
    MM(xf0, wf0, xh0);
    ILV();
    SBAR();
    MM(xf1, wf1, xh1);
#undef ILV
#undef RD
#undef MM
    STAMP(3);
    LBAR();
    ldsp cb = lds + g * 8 + l15 * CSTR;
#pragma unroll
    for (int i = 0; i < 8; ++i) {
#pragma unroll
        for (int a = 0; a < 4; ++a) {
            if (i < 7 || a < 2) {
                u32x2 v = {pk2(acc[i][a][0], acc[i][a][1]), pk2(acc[i][a][2], acc[i][a][3])};
                *(LAS u32x2*)(cb + M::ntile(i) * 32 + (16 + 16 * M::mtile(a)) * CSTR) = v;
            }
        }
        if (ISB && i < 3 - M::WM) {
            u32x2 v = {pk2(hacc[i][0], hacc[i][1]), pk2(hacc[i][2], hacc[i][3])};
            *(LAS u32x2*)(cb + M::ntile(i) * 32) = v;
        }
    }
    if (GATE && gout != nullptr) {
        const float z = rbf(g == 0 ? hacc[0][0] : (g == 1 ? hacc[1][0] : (g == 2 ? hacc[2][0] : hacc[3][0])));
        __hip_atomic_store(gout + 64 * M::WM + lane, rbf(__builtin_amdgcn_rcpf(1.f + __builtin_amdgcn_exp2f(-z * LOG2E))), __ATOMIC_RELAXED, __HIP_MEMORY_SCOPE_AGENT);
    }
}

template <int LW>
__device__ __forceinline__ void k1_loader(unsigned ldsb, int lane, const char* x, const char* wsrc, bool isB, int r0, int hg, int koff,
                                          const char* gatew, const char* wphi, const char* woA, const char* woB, int b, const __amdgpu_buffer_rsrc_t wrs STAMP_ARG) {
    if (LW < 2) { const unsigned c = 64u * LW + lane; glds16(gatew, (c < 80u ? c : 79u) * 16u, ldsb + L_GW + LW * 1024); }
    const unsigned xo = (unsigned)((lane >> 3) * 1280 + (((lane & 7) ^ (lane >> 3)) * 16));
    const char* xb = x + (size_t)(r0 - 16) * 1280;
    const char* xh = x + (size_t)(r0 >= 16 ? r0 - 16 : 0) * 1280;
    unsigned wo[5];
    if (LW > 0) {
#pragma unroll
        for (int q = 0; q < 5; ++q) {
            const unsigned c = 64u * q + lane;
            const unsigned rho = (c * 205u) >> 11, ch = c - rho * 10u;
            wo[q] = (unsigned)krow_inv((int)rho) * 3840u + ch * 16u;
        }
    }
    const char* wbase = wsrc + hg * 160;
    const int sc0 = isB ? 1280 : 0, sc1 = isB ? 0 : 1280;
    auto issue = [&](int s, unsigned buf) {
        int ks = s + koff; ks = ks >= 10 ? ks - 10 : ks;
#pragma unroll
        for (int i = 0; i < 12; ++i) {
            const int p = 12 * LW + i;
            if (p < 18) {
                const char* sb = (p < 2 ? xh : xb) + ks * 128 + p * 10240;
                glds16(sb, xo, ldsb + buf + p * 1024);
            } else {
                const int q = p - 18, seg = q / 10, hf = (q % 10) / 5;
                const char* sb = wbase + (size_t)ks * (64 * 3840) + hf * (32 * 3840) + (seg == 0 ? sc0 : (seg == 1 ? sc1 : 2560));
                glds16(sb, wo[q % 5], ldsb + buf + p * 1024);
            }
        }
    };
    unsigned cur = 0;
#pragma unroll 1
    for (int s = 0; s < 10; ++s) {
        if (s >= 2) VBAR(12);
        issue(s, cur);
        cur = (cur == 2 * ST) ? 0u : cur + ST;
        if (s == 5) {
            const u32x4 sent = {0xffffffffu, 0xffffffffu, 0xffffffffu, 0xffffffffu};
            const unsigned dm = (unsigned)WS_O + ((b >> 3) * 8 + 4 + ((b >> 1) & 3)) * 20480 + (b & 1) * 10240 + LW * 1024 + lane * 16;
            ST128(wrs, dm, sent); ST128(wrs, dm + 4096, sent);
            if (LW < 2) ST128(wrs, dm + 8192, sent);
        }
        if (s == 3) {
            const unsigned c = 64u * LW + lane;
            glds16(wphi + hg * 3200, (c < 200u ? c : 199u) * 16u, ldsb + L_WPH + LW * 1024);
            const int bb = b < 200 ? b : 199;
            const int kb = bb / 5, ng = bb - kb * 5, st = kb / 5, j = kb - st * 5;
            const char* wo = ((st >> 2) ? woA : woB) + (size_t)(8 * (st & 3)) * 20 * 1280 + ng * 256;
#pragma unroll
            for (int i = 0; i < 2; ++i) {
                const int kk = 8 * LW + 4 * i + (lane >> 4), e = kk & 7;
                const int head = j < 4 ? 2 * j + (LW >> 1) : 2 * LW + (e >> 2);
                const int d = j < 4 ? 8 * (LW & 1) + e : 16 + (e & 3);
                glds16(wo, (unsigned)((head * 20 + d) * 1280 + (lane & 15) * 16), ldsb + L_WO + (2 * LW + i) * 1024);
            }
        }
    }
}

__global__ __launch_bounds__(512, 2) void k1_qkv(const __hip_bfloat16* __restrict__ x, const __hip_bfloat16* __restrict__ wA,
                                                   const __hip_bfloat16* __restrict__ wB, const __hip_bfloat16* __restrict__ wphi,
                                                   const __hip_bfloat16* __restrict__ gatew, const __hip_bfloat16* __restrict__ woA,
                                                   const __hip_bfloat16* __restrict__ woB, char* __restrict__ ws) {
    __shared__ __attribute__((aligned(16))) char smem[K1_LDS_ALL];
    ldsp lds = (ldsp)smem;
    STAMP_DECL STAMP(0);
    const int tid = threadIdx.x, lane = tid & 63;
    const int w = __builtin_amdgcn_readfirstlane(tid >> 6);
    const int b = blockIdx.x, xcd = b & 7, idx = b >> 3;
    const int rt = 8 * (xcd & 1) + (idx & 7);
    const int cg = 4 * (xcd >> 1) + (idx >> 3);
    const bool isB = cg >= 8;
    const int hg = cg & 7;
    const int r0 = rt * 128;
    const int r32 = lane & 31, hh = lane >> 5;
    const __amdgpu_buffer_rsrc_t wrs = WSRSRC(ws);
    int koff = 3 * (idx >> 3) + (idx & 7); koff = koff >= 10 ? koff - 10 : koff;

    if (w < 4) {
        float* gout = cg == 0 ? (float*)(ws + WS_G) + r0 : nullptr;
        STAMP(1);
        if (isB) {
            if (w == 0) k1_compute<0, true>(lds, lane, koff, gout STAMP_PASS); else if (w == 1) k1_compute<1, true>(lds, lane, koff, gout STAMP_PASS);
            else if (w == 2) k1_compute<2, true>(lds, lane, koff, gout STAMP_PASS); else k1_compute<3, true>(lds, lane, koff, gout STAMP_PASS);
        } else {
            if (w == 0) k1_compute<0, false>(lds, lane, koff, gout STAMP_PASS); else if (w == 1) k1_compute<1, false>(lds, lane, koff, gout STAMP_PASS);
            else if (w == 2) k1_compute<2, false>(lds, lane, koff, gout STAMP_PASS); else k1_compute<3, false>(lds, lane, koff, gout STAMP_PASS);
        }
    } else {
        const unsigned ldsb = (unsigned)(uintptr_t)lds;
        const char* wsrc = (const char*)(isB ? wB : wA);
        if (w == 4) k1_loader<0>(ldsb, lane, (const char*)x, wsrc, isB, r0, hg, koff, (const char*)gatew, (const char*)wphi, (const char*)woA, (const char*)woB, b, wrs STAMP_PASS);
        else if (w == 5) k1_loader<1>(ldsb, lane, (const char*)x, wsrc, isB, r0, hg, koff, (const char*)gatew, (const char*)wphi, (const char*)woA, (const char*)woB, b, wrs STAMP_PASS);
        else if (w == 6) k1_loader<2>(ldsb, lane, (const char*)x, wsrc, isB, r0, hg, koff, (const char*)gatew, (const char*)wphi, (const char*)woA, (const char*)woB, b, wrs STAMP_PASS);
        else k1_loader<3>(ldsb, lane, (const char*)x, wsrc, isB, r0, hg, koff, (const char*)gatew, (const char*)wphi, (const char*)woA, (const char*)woB, b, wrs STAMP_PASS);
        VBAR(12);
        VBAR(0);
        STAMP(3);
        LBAR();
    }
    LBAR();
    STAMP(4);
    if (b < 200) {
        const int kb = b / 5, ng = b - kb * 5, st = kb / 5, j = kb - st * 5;
        const int nt = 8 * ng + w, ct = nt / 5, n = nt - ct * 5;
        ldsp pw = lds + L_WO + (8 * (lane >> 4) + ((lane >> 2) & 3)) * 256 + w * 32 + (lane & 3) * 8;
        const u32x2 f0 = lds_tr(pw), f1 = lds_tr(pw + 1024);
        ST128(wrs, (unsigned)WS_WOP + (((ct * 8 + st) * 5 + j) * 5 + n) * 1024 + lane * 16, (u32x4{f0[0], f0[1], f1[0], f1[1]}));
    }

    if (!isB) {
        const int hl = w & 3, rp = w >> 2;
        const int h = 4 * hg + hl;
        const u32x2 z2 = {0u, 0u};
        bf16x8 wa0, wa1;
        {
            const int rho = (r32 & 3) + 4 * (r32 >> 3), ho = (r32 >> 2) & 1;
            const bool vr = rho < 10, v1 = vr && hh == 0;
            const LAS unsigned short* wpp = (const LAS unsigned short*)(lds + L_WPH + hl * 800) + (vr ? 10 * ho + rho : 0);
            u32x4 t0, t1;
#pragma unroll
            for (int j = 0; j < 4; ++j) { const unsigned v = (unsigned)wpp[(8 * hh + 2 * j) * 20] | ((unsigned)wpp[(8 * hh + 2 * j + 1) * 20] << 16); t0[j] = vr ? v : 0u; }
#pragma unroll
            for (int j = 0; j < 2; ++j) { const unsigned v = (unsigned)wpp[(16 + 2 * j) * 20] | ((unsigned)wpp[(17 + 2 * j) * 20] << 16); t1[j] = v1 ? v : 0u; }
            t1[2] = 0u; t1[3] = 0u;
            wa0 = __builtin_bit_cast(bf16x8, t0); wa1 = __builtin_bit_cast(bf16x8, t1);
            if (rt == 0 && rp == 0) {
                const unsigned dst = (unsigned)WS_WPF + h * 2048 + lane * 16;
                ST128(wrs, dst, t0);
                ST128(wrs, dst + 1024, t1);
            }
        }
        f32x16 p[2];
#pragma unroll
        for (int u = 0; u < 2; ++u) {
            ldsp cp = lds + (16 + 32 * (2 * rp + u) + r32) * CSTR + (80 + 20 * hl) * 2;
            bf16x8 b0 = cat2(lds_b64(cp + hh * 16), lds_b64(cp + hh * 16 + 8));
            u32x2 t = lds_b64(cp + 32);
            if (hh) t = z2;
            p[u] = MFMA32(wa0, b0, (f32x16{}));
            p[u] = MFMA32(wa1, cat2(t, z2), p[u]);
        }
#pragma unroll
        for (int u = 0; u < 2; ++u) {
            ldsp cp = lds + (16 + 32 * (2 * rp + u) + r32) * CSTR + (20 * hl) * 2;
            u32x2 a0 = lds_b64(cp + hh * 16), a1 = lds_b64(cp + hh * 16 + 8), t = lds_b64(cp + 32);
            const unsigned dst = (unsigned)WS_QF + (h * 64 + (rt * 4 + 2 * rp + u)) * 1280;
            ST128(wrs, dst + lane * 16, (u32x4{a0[0], a0[1], a1[0], a1[1]}));
            if (hh == 0) ST64(wrs, dst + 1024 + r32 * 8, t);
        }
#pragma unroll
        for (int u = 0; u < 2; ++u) {
            unsigned o[10];
            feat_map(p[u], o);
            ldsp pp = lds + L_PHIK + (32 * (2 * rp + u) + r32) * PHSTR + hl * 80 + hh * 40;
#pragma unroll
            for (int i = 0; i < 5; ++i) *(LAS u32x2*)(pp + 8 * i) = u32x2{o[2 * i], o[2 * i + 1]};
        }
        LBAR();
        STAMP(5);
        {
            const int nt = w >> 2;
            const int g = lane >> 4, q = (lane >> 2) & 3, pp = lane & 3, l15 = lane & 15;
            const int rowl = 4 * (4 * (g & 1) + q) + 2 * (g >> 1);
            ldsp pa = lds + L_PHIK + rowl * PHSTR + hl * 80 + pp * 8;
            ldsp pv = lds + (16 + rowl) * CSTR + (160 + 20 * hl + 16 * nt + 4 * pp) * 2;
            f32x4 kv[3];
#pragma unroll
            for (int m = 0; m < 3; ++m) kv[m] = f32x4{0.f, 0.f, 0.f, 0.f};
            const bool ones = (nt == 1) && (l15 == 4);
#pragma unroll
            for (int kk = 0; kk < 4; ++kk) {
                u32x2 v0 = lds_tr(pv + kk * 32 * CSTR), v1 = lds_tr(pv + kk * 32 * CSTR + CSTR);
                if (ones) { v0 = u32x2{0x3f803f80u, 0x3f803f80u}; v1 = v0; }
                bf16x8 vb = cat2(v0, v1);
#pragma unroll
                for (int m = 0; m < 3; ++m) {
                    bf16x8 af = cat2(lds_tr(pa + kk * 32 * PHSTR + m * 32), lds_tr(pa + kk * 32 * PHSTR + PHSTR + m * 32));
                    kv[m] = MFMA16(af, vb, kv[m]);
                }
            }
            const int d = 16 * nt + l15;
            const unsigned dst = (unsigned)WS_KVP + ((rt * 32 + h) * 840 + d * 40 + 4 * g) * 4;
            if (d < 21) {
                ST128(wrs, dst, __builtin_bit_cast(u32x4, kv[0]));
                ST128(wrs, dst + 64, __builtin_bit_cast(u32x4, kv[1]));
                if (g < 2) ST128(wrs, dst + 128, __builtin_bit_cast(u32x4, kv[2]));
            }
        }
    } else {
        const int hl = w & 3;
        const int row = 64 * (w >> 2) + lane;
        const int srow = r0 + row;
        ldsp base = lds + (16 + row) * CSTR + 40 * hl;
        unsigned qv[10];
#pragma unroll
        for (int i = 0; i < 5; ++i) { u32x2 t = lds_b64(base + 160 + 8 * i); qv[2 * i] = t[0]; qv[2 * i + 1] = t[1]; }
        float lg[8];
        const float scale = 0.22360679774997896f;
#pragma unroll
        for (int j = 0; j < 8; ++j) {
            ldsp kp = base - 2 * j * CSTR;
            float a0 = 0.f, a1 = 0.f;
#pragma unroll
            for (int i = 0; i < 5; ++i) { u32x2 t = lds_b64(kp + 8 * i); a0 = dot2(qv[2 * i], t[0], a0); a1 = dot2(qv[2 * i + 1], t[1], a1); }
            lg[j] = (srow - 2 * j >= 0) ? (a0 + a1) * scale : -1e9f;
        }
        float mx = fmaxf(fmaxf(lg[0], lg[1]), lg[2]);
        mx = fmaxf(fmaxf(mx, lg[3]), lg[4]); mx = fmaxf(fmaxf(mx, lg[5]), lg[6]); mx = fmaxf(mx, lg[7]);
        const float mxl = -mx * LOG2E;
        float sum = 0.f;
#pragma unroll
        for (int j = 0; j < 8; ++j) { lg[j] = __builtin_amdgcn_exp2f(fmaf(lg[j], LOG2E, mxl)); sum += lg[j]; }
        const float rs = __builtin_amdgcn_rcpf(sum);
        float o[20];
#pragma unroll
        for (int i = 0; i < 20; ++i) o[i] = 0.f;
#pragma unroll
        for (int jp = 0; jp < 4; ++jp) {
            const unsigned pw = pk2(lg[2 * jp] * rs, lg[2 * jp + 1] * rs);
            ldsp va = base + 320 - 4 * jp * CSTR;
            ldsp vb = va - 2 * CSTR;
#pragma unroll
            for (int i = 0; i < 5; ++i) {
                u32x2 ta = lds_b64(va + 8 * i), tb = lds_b64(vb + 8 * i);
#pragma unroll
                for (int e = 0; e < 2; ++e) {
                    const unsigned lo = __builtin_amdgcn_perm(tb[e], ta[e], 0x05040100u);
                    const unsigned hi = __builtin_amdgcn_perm(tb[e], ta[e], 0x07060302u);
                    o[4 * i + 2 * e] = dot2(pw, lo, o[4 * i + 2 * e]);
                    o[4 * i + 2 * e + 1] = dot2(pw, hi, o[4 * i + 2 * e + 1]);
                }
            }
        }
        STAMP(5);
        const int hB = 4 * hg + hl;
        const unsigned ob = (unsigned)WS_O + ((srow >> 6) * 8 + (hB >> 3)) * 20480;
        const unsigned dm = ob + (hB & 7) * 2048 + lane * 32;
        ST128(wrs, dm, (u32x4{pk2(o[0], o[1]), pk2(o[2], o[3]), pk2(o[4], o[5]), pk2(o[6], o[7])}));
        ST128(wrs, dm + 16, (u32x4{pk2(o[8], o[9]), pk2(o[10], o[11]), pk2(o[12], o[13]), pk2(o[14], o[15])}));
        ST64(wrs, ob + 16384 + (hB & 7) * 512 + lane * 8, (u32x2{pk2(o[16], o[17]), pk2(o[18], o[19])}));
    }
    STAMP_OUT(0);
}

constexpr int S2_M = 16384, S2_O = 20480, S2_W = 25600;
constexpr int K2_W = 4 * S2_O;
constexpr int K2_KV = K2_W + 3 * S2_W;
constexpr int K2_LDS = K2_KV + 2048;

constexpr int K2_FL = K2_KV + 1792;
__device__ __forceinline__ void lds_add1(ldsp p, int lane) {
    if (lane == 0) __hip_atomic_fetch_add((LAS unsigned*)p, 1u, __ATOMIC_RELAXED, __HIP_MEMORY_SCOPE_WORKGROUP);
}
__device__ __forceinline__ void lds_wait4(ldsp p) {
    while ((unsigned)__builtin_amdgcn_readfirstlane(__hip_atomic_load((LAS unsigned*)p, __ATOMIC_RELAXED, __HIP_MEMORY_SCOPE_WORKGROUP)) < 4u) __builtin_amdgcn_s_sleep(1);
    asm volatile("" ::: "memory");
}

__device__ __forceinline__ void k2_oloader(ldsp lds, int lane, const char* osrc, int lw, int so STAMP_ARG) {
    const unsigned ldsb = (unsigned)(uintptr_t)lds + lw * 10240;
    const unsigned lo = (unsigned)lane * 16u;
    auto issue = [&](int sp_) {
        const int sp = __builtin_amdgcn_readfirstlane(sp_);
        const unsigned dst = ldsb + (unsigned)(sp & 3) * S2_O;
        const char* s = osrc + ((sp & 4) | ((sp + so) & 3)) * 20480;
        gcopy<4, true>(s, lo, dst); gcopy<4, true>(s + 4096, lo, dst + 4096); gcopy<2, true>(s + 8192, lo, dst + 8192);
    };
    int s = 0;
    int redo = 0;
#pragma unroll 1
    for (int j = 0; j < 8;) {
#pragma unroll 1
        for (; s <= j + 1 && s < 8; ++s) {
            if (s >= 4) lds_wait4(lds + K2_FL + 32 + 4 * (s - 4));
            issue(s);
        }
        if (j < 7) asm volatile("s_waitcnt vmcnt(10)" ::: "memory"); else asm volatile("s_waitcnt vmcnt(0)" ::: "memory");
        if (j >= 4 && redo < (1 << 16)) {
            u32x4 v[10];
            ldsp p = lds + lw * 10240 + (j & 3) * S2_O + lo;
#pragma unroll
            for (int i = 0; i < 10; ++i) v[i] = *(const LAS u32x4*)(p + i * 1024);
            unsigned mx = 0u;
#pragma unroll
            for (int i = 0; i < 10; ++i) { mx = max(max(mx, v[i][0]), v[i][1]); mx = max(max(mx, v[i][2]), v[i][3]); }
            if (__builtin_amdgcn_ballot_w64(mx == 0xffffffffu) != 0ull) {
                ++redo;
                asm volatile("s_waitcnt vmcnt(0)" ::: "memory");
                s = j;
                continue;
            }
        }
        lds_add1(lds + K2_FL + 4 * j, lane);
        if (j == 0) STAMP(2); if (j == 3) STAMP(3); if (j == 4) STAMP(4); if (j == 7) STAMP(5);
        ++j;
    }
}
__device__ __forceinline__ void k2_wloader(ldsp lds, int lane, const char* wsrc, int lw, int so) {
    const unsigned ldsb = (unsigned)(uintptr_t)lds + K2_W + lw * 12288;
    const unsigned lo = (unsigned)lane * 16u;
    unsigned cur = 0;
#pragma unroll 1
    for (int s = 0; s < 9; ++s) {
        if (s < 8) {
            if (s >= 3) lds_wait4(lds + K2_FL + 32 + 4 * (s - 3));
            const char* p = wsrc + ((s & 4) | ((s + so) & 3)) * 25600;
            const unsigned dst = ldsb + cur;
            gcopy<4, false>(p, lo, dst); gcopy<4, false>(p + 4096, lo, dst + 4096); gcopy<4, false>(p + 8192, lo, dst + 8192);
            gcopy<1, false>(p + 12288, lo, dst + 12288);
            cur = (cur == 2 * S2_W) ? 0u : cur + S2_W;
        }
        if (s >= 1) {
            if (s < 8) asm volatile("s_waitcnt vmcnt(13)" ::: "memory"); else asm volatile("s_waitcnt vmcnt(0)" ::: "memory");
            lds_add1(lds + K2_FL + 4 * (s - 1), lane);
        }
    }
}

__global__ __launch_bounds__(768, 3) void k2_out(char* __restrict__ ws, __hip_bfloat16* __restrict__ out) {
    __shared__ __attribute__((aligned(16))) char smem[K2_LDS + 1024];
    ldsp lds = (ldsp)smem;
    STAMP_DECL STAMP(0);
    const int tid = threadIdx.x, lane = tid & 63;
    const int w = __builtin_amdgcn_readfirstlane(tid >> 6);
    const int b = blockIdx.x, xcd = b & 7, idx = b >> 3;
    const int rt = 8 * (xcd & 3) + (idx & 7);
    const int ct = 4 * (xcd >> 2) + (idx >> 3);
    const int r0 = rt * 64;
    const int g = lane >> 4, l15 = lane & 15, r32 = lane & 31, hh = lane >> 5;
    const u32x2 z2 = {0u, 0u};
    if (w < 4) {
        const int h = b & 31, c = b >> 5;
        u32x4 q0[2]; u32x2 q1[2];
#pragma unroll
        for (int u = 0; u < 2; ++u) {
            const char* pq = ws + WS_QF + ((size_t)h * 64 + c * 8 + 2 * w + u) * 1280;
            q0[u] = *(const u32x4*)(pq + lane * 16);
            q1[u] = *(const u32x2*)(pq + 1024 + r32 * 8);
        }
        const u32x4 wa0 = *(const u32x4*)(ws + WS_WPF + (size_t)h * 2048 + lane * 16);
        const u32x4 wa1 = *(const u32x4*)(ws + WS_WPF + (size_t)h * 2048 + 1024 + lane * 16);
        f32x4 t[16];
        {
            const int tc = tid < 210 ? tid : 209;
            const char* kp = ws + WS_KVP + (size_t)h * 3360 + tc * 16;
#pragma unroll
            for (int r = 0; r < 16; ++r) t[r] = *(const f32x4*)(kp + (size_t)r * (32 * 3360));
        }
        if (tid < 32) *(volatile LAS unsigned*)(lds + K2_FL + tid * 4) = 0u;
        LBAR();
        unsigned ph[2][10];
#pragma unroll
        for (int u = 0; u < 2; ++u) {
            u32x2 tq = q1[u]; if (hh) tq = z2;
            f32x16 p = MFMA32(__builtin_bit_cast(bf16x8, wa0), __builtin_bit_cast(bf16x8, q0[u]), (f32x16{}));
            p = MFMA32(__builtin_bit_cast(bf16x8, wa1), cat2(tq, z2), p);
            feat_map(p, ph[u]);
        }
        SBAR();
        const f32x4 ks = (((t[0] + t[1]) + (t[2] + t[3])) + ((t[4] + t[5]) + (t[6] + t[7]))) + (((t[8] + t[9]) + (t[10] + t[11])) + ((t[12] + t[13]) + (t[14] + t[15])));
        if (tid < 210) *(LAS u32x2*)(lds + K2_KV + tid * 8) = u32x2{pk2(ks[0], ks[1]), pk2(ks[2], ks[3])};
        asm volatile("s_waitcnt lgkmcnt(0)" ::: "memory");
        lds_add1(lds + K2_FL + 64, lane);
        STAMP(1);
        lds_wait4(lds + K2_FL + 64);
        const int rho = (r32 & 3) + 4 * (r32 >> 3), ho = (r32 >> 2) & 1;
        const bool vr = rho <= 10;
        const int d = rho < 8 ? 8 * ho + rho : (rho < 10 ? 16 + 2 * ho + (rho - 8) : 20);
        ldsp ap = lds + K2_KV + d * 80 + hh * 40;
        u32x2 a00 = lds_b64(ap), a01 = lds_b64(ap + 8), a10 = lds_b64(ap + 16), a11 = lds_b64(ap + 24), a20 = lds_b64(ap + 32);
        if (!vr) { a00 = z2; a01 = z2; a10 = z2; a11 = z2; a20 = z2; }
        const __amdgpu_buffer_rsrc_t rs = __builtin_amdgcn_make_buffer_rsrc(ws + WS_O + (size_t)((c * 4 + w) * 8 + 4 + (h >> 3)) * 20480, 0, 20480, 0x00020000);
#pragma unroll
        for (int u = 0; u < 2; ++u) {
            f32x16 p = MFMA32(cat2(a00, a01), cat2(u32x2{ph[u][0], ph[u][1]}, u32x2{ph[u][2], ph[u][3]}), (f32x16{}));
            p = MFMA32(cat2(a10, a11), cat2(u32x2{ph[u][4], ph[u][5]}, u32x2{ph[u][6], ph[u][7]}), p);
            p = MFMA32(cat2(a20, z2), cat2(u32x2{ph[u][8], ph[u][9]}, z2), p);
            const float rd = __builtin_amdgcn_rcpf(p[10] + 1e-6f);
            const int srow = u * 32 + r32;
            u32x4 m = {pk2(p[0] * rd, p[1] * rd), pk2(p[2] * rd, p[3] * rd), pk2(p[4] * rd, p[5] * rd), pk2(p[6] * rd, p[7] * rd)};
            __builtin_amdgcn_raw_buffer_store_b128(m, rs, (unsigned)((h & 7) * 2048 + srow * 32 + hh * 16), 0, 16);
            __builtin_amdgcn_raw_buffer_store_b32(pk2(p[8] * rd, p[9] * rd), rs, (unsigned)(16384 + (h & 7) * 512 + srow * 8 + hh * 4), 0, 16);
        }
        STAMP(2);
    } else if (w < 8) {
        const int so = STAG ? ((idx & 7) + (idx >> 3)) & 3 : 0;
        asm volatile("s_barrier" ::: "memory");
        if (w < 6) k2_oloader(lds, lane, ws + WS_O + (size_t)rt * (8 * 20480) + (w - 4) * 10240, w - 4, so STAMP_PASS);
        else k2_wloader(lds, lane, ws + WS_WOP + (size_t)ct * (8 * 25600) + (w - 6) * 12288, w - 6, so);
    } else {
        const int cw = w - 8;
        const float gv = ((const float*)(ws + WS_G))[r0 + 16 * cw + l15];
        asm volatile("s_barrier" ::: "memory");
        const unsigned xm = (unsigned)((g >> 1) * 2048 + (16 * cw + l15) * 32 + (g & 1) * 16);
        const unsigned xt = (unsigned)(S2_M + 2 * g * 512 + (16 * cw + l15) * 8);
        const unsigned wa = (unsigned)(K2_W + lane * 16);
        f32x4 accb[5], acca[5];
#pragma unroll
        for (int n = 0; n < 5; ++n) { accb[n] = f32x4{0.f, 0.f, 0.f, 0.f}; acca[n] = accb[n]; }
        unsigned cur = 0;
#define STEP(ACC, ST_, J_) do { \
            lds_wait4(lds + K2_FL + 4 * (J_)); \
            ldsp po = lds + (ST_) * S2_O; ldsp pw = lds + cur + wa; \
            bf16x8 xf[5], wf[5][5]; \
            _Pragma("unroll") for (int j = 0; j < 5; ++j) { \
                xf[j] = j < 4 ? lds_b128(po + xm + j * 4096) : cat2(lds_b64(po + xt), lds_b64(po + xt + 512)); \
                _Pragma("unroll") for (int n = 0; n < 5; ++n) \
                    wf[j][n] = lds_b128(pw + j * 5120 + n * 1024); } \
            _Pragma("unroll") for (int j = 0; j < 5; ++j) \
                _Pragma("unroll") for (int n = 0; n < 5; ++n) ACC[n] = MFMA16(wf[j][n], xf[j], ACC[n]); \
            __builtin_amdgcn_sched_group_barrier(0x100, 12, 0); \
            _Pragma("unroll") for (int t_ = 0; t_ < 19; ++t_) { \
                __builtin_amdgcn_sched_group_barrier(0x008, 1, 0); __builtin_amdgcn_sched_group_barrier(0x100, 1, 0); } \
            __builtin_amdgcn_sched_group_barrier(0x008, 6, 0); \
            SBAR(); \
            asm volatile("s_waitcnt lgkmcnt(0)" ::: "memory"); \
            lds_add1(lds + K2_FL + 32 + 4 * (J_), lane); \
            cur = (cur == 2 * S2_W) ? 0u : cur + S2_W; \
        } while (0)
#pragma unroll 1
        for (int st = 0; st < 4; ++st) { STEP(accb, st, st); if (st == 0) STAMP(3); }
        STAMP(4);
#pragma unroll 1
        for (int st = 0; st < 4; ++st) STEP(acca, st, st + 4);
#undef STEP
        STAMP(5);
        const float gb = rbf(1.f - gv);
        char* dst = (char*)out + (size_t)(r0 + 16 * cw + l15) * 1280 + ct * 160 + g * 8;
#pragma unroll
        for (int n = 0; n < 5; ++n) {
            float y[4];
#pragma unroll
            for (int e = 0; e < 4; ++e) y[e] = rbf(gv * rbf(acca[n][e])) + rbf(gb * rbf(accb[n][e]));
            *(u32x2*)(dst + n * 32) = u32x2{pk2(y[0], y[1]), pk2(y[2], y[3])};
        }
    }
}

extern "C" void kernel_launch(void* const* d_in, const int* in_sizes, int n_in,
                              void* d_out, int out_size, void* d_ws, size_t ws_size,
                              hipStream_t stream) {
    const __hip_bfloat16* x    = (const __hip_bfloat16*)d_in[0];
    const __hip_bfloat16* wA   = (const __hip_bfloat16*)d_in[1];
    const __hip_bfloat16* wphi = (const __hip_bfloat16*)d_in[2];
    const __hip_bfloat16* woA  = (const __hip_bfloat16*)d_in[3];
    const __hip_bfloat16* wB   = (const __hip_bfloat16*)d_in[5];
    const __hip_bfloat16* woB  = (const __hip_bfloat16*)d_in[6];
    const __hip_bfloat16* gw   = (const __hip_bfloat16*)d_in[7];
    char* ws = (char*)d_ws;
    k1_qkv<<<256, 512, 0, stream>>>(x, wA, wB, wphi, gw, woA, woB, ws);
    k2_out<<<256, 768, 0, stream>>>(ws, (__hip_bfloat16*)d_out);
}
```

```cpp
#include <hip/hip_runtime.h>
#include <hip/hip_bf16.h>
#include <stdint.h>

typedef __bf16 bf16x8 __attribute__((ext_vector_type(8)));
typedef __bf16 bf16x2 __attribute__((ext_vector_type(2)));
typedef short s16x4 __attribute__((ext_vector_type(4)));
typedef float f32x2 __attribute__((ext_vector_type(2)));
typedef float f32x4 __attribute__((ext_vector_type(4)));
typedef float f32x16 __attribute__((ext_vector_type(16)));
typedef unsigned u32x2 __attribute__((ext_vector_type(2)));
typedef unsigned u32x4 __attribute__((ext_vector_type(4)));
#define LAS __attribute__((address_space(3)))
typedef LAS char* ldsp;

constexpr size_t WS_QF  = 0;
constexpr size_t WS_O   = WS_QF + 32ull * 64 * 1280;
constexpr size_t WS_KVP = WS_O + 32ull * 8 * 20480;
constexpr size_t WS_WOP = WS_KVP + 16ull * 32 * 840 * 4;
constexpr size_t WS_WPF = WS_WOP + 64ull * 25600;
constexpr size_t WS_G   = WS_WPF + 32ull * 64 * 32;
constexpr size_t WS_CNT = WS_G + 2048 * 4;

constexpr int ST_X = 144 * 128, ST_W = 3 * 64 * 160, ST = ST_X + ST_W;
constexpr int K1_LDS = 3 * ST;
constexpr int CSTR = 488;
constexpr int PHSTR = 328;
constexpr int L_PHIK = 144 * CSTR;
constexpr int L_GW = K1_LDS;
constexpr int L_WPH = L_GW + 2048;
constexpr int L_WO = L_WPH + 4096;
constexpr int K1_LDS_ALL = L_WO + 8192;
constexpr int L_FL = K1_LDS_ALL + 256;

#define STAMP(i)
#define STAMP_DECL
#define STAMP_OUT(kid)
#define STAMP_ARG
#define STAMP_PASS

#define SBAR() __builtin_amdgcn_sched_barrier(0)
#define VBAR(N) asm volatile("s_waitcnt vmcnt(%0)\n\ts_barrier" :: "i"(N) : "memory")
#define LBAR() asm volatile("s_waitcnt lgkmcnt(0)\n\ts_barrier" ::: "memory")
#define MFMA16(a, b, c) __builtin_amdgcn_mfma_f32_16x16x32_bf16(a, b, c, 0, 0, 0)
#define MFMA32(a, b, c) __builtin_amdgcn_mfma_f32_32x32x16_bf16(a, b, c, 0, 0, 0)

__device__ __forceinline__ void glds16(const char* sbase, unsigned voff, unsigned lds_dst) {
    unsigned sv;
    asm volatile("s_mov_b32 %0, m0\n\ts_mov_b32 m0, %3\n\ts_nop 0\n\tglobal_load_lds_dwordx4 %1, %2\n\ts_mov_b32 m0, %0"
                 : "=&s"(sv) : "v"(voff), "s"(sbase), "s"(lds_dst) : "memory");
}
__device__ __forceinline__ void glds16_sc1(const char* sbase, unsigned voff, unsigned lds_dst) {
    unsigned sv;
    asm volatile("s_mov_b32 %0, m0\n\ts_mov_b32 m0, %3\n\ts_nop 0\n\tglobal_load_lds_dwordx4 %1, %2 sc1\n\ts_mov_b32 m0, %0"
                 : "=&s"(sv) : "v"(voff), "s"(sbase), "s"(lds_dst) : "memory");
}
#define EPF 1
#define KST 0
#define CPF 1
#define BDELAY 0
#define STAG 1
#define WPF 1
#define OSC1 4
#define K1AUX 16
#define WSRSRC(ws) __builtin_amdgcn_make_buffer_rsrc((void*)(ws), 0, (int)(WS_CNT + 4096), 0x00020000)
#define ST128(rs, off, v) __builtin_amdgcn_raw_buffer_store_b128((v), rs, (unsigned)(off), 0, K1AUX)
#define ST64(rs, off, v) __builtin_amdgcn_raw_buffer_store_b64((v), rs, (unsigned)(off), 0, K1AUX)
#define ST32(rs, off, v) __builtin_amdgcn_raw_buffer_store_b32((v), rs, (unsigned)(off), 0, K1AUX)
template <int N, bool SC1>
__device__ __forceinline__ void gcopy(const char* sbase, unsigned voff, unsigned lds_dst) {
    unsigned sv;
#define GL_(o) "global_load_lds_dwordx4 %1, %2 offset:" #o
#define GC_(body) asm volatile("s_mov_b32 %0, m0\n\ts_mov_b32 m0, %3\n\ts_nop 0\n\t" body "s_mov_b32 m0, %0" : "=&s"(sv) : "v"(voff), "s"(sbase), "s"(lds_dst) : "memory")
    if constexpr (SC1) {
        if constexpr (N == 4) GC_(GL_(0) " sc1\n\t" GL_(1024) " sc1\n\t" GL_(2048) " sc1\n\t" GL_(3072) " sc1\n\t");
        else if constexpr (N == 2) GC_(GL_(0) " sc1\n\t" GL_(1024) " sc1\n\t");
        else GC_(GL_(0) " sc1\n\t");
    } else {
        if constexpr (N == 4) GC_(GL_(0) "\n\t" GL_(1024) "\n\t" GL_(2048) "\n\t" GL_(3072) "\n\t");
        else if constexpr (N == 2) GC_(GL_(0) "\n\t" GL_(1024) "\n\t");
        else GC_(GL_(0) "\n\t");
    }
#undef GL_
#undef GC_
}
__device__ __forceinline__ bf16x8 lds_b128(ldsp p) { return *(const LAS bf16x8*)p; }
__device__ __forceinline__ u32x2 lds_b64(ldsp p) { return *(const LAS u32x2*)p; }
__device__ __forceinline__ u32x2 lds_tr(ldsp p) {
    return __builtin_bit_cast(u32x2, __builtin_amdgcn_ds_read_tr16_b64_v4i16((LAS s16x4*)p));
}
__device__ __forceinline__ bf16x8 cat2(u32x2 a, u32x2 b) {
    u32x4 r = {a[0], a[1], b[0], b[1]};
    return __builtin_bit_cast(bf16x8, r);
}
__device__ __forceinline__ unsigned pk2(float lo, float hi) {
    bf16x2 r = {(__bf16)lo, (__bf16)hi};
    return __builtin_bit_cast(unsigned, r);
}
__device__ __forceinline__ float rbf(float x) { return (float)(__bf16)x; }
__device__ __forceinline__ float bflo(unsigned u) { return __uint_as_float(u << 16); }
__device__ __forceinline__ float bfhi(unsigned u) { return __uint_as_float(u & 0xffff0000u); }
__device__ __forceinline__ float dot2(unsigned a, unsigned b, float c) {
    return __builtin_amdgcn_fdot2_f32_bf16(__builtin_bit_cast(bf16x2, a), __builtin_bit_cast(bf16x2, b), c, false);
}
#define XHALF(v, A, B) do { auto r_ = __builtin_amdgcn_permlane32_swap(__float_as_uint(v), __float_as_uint(v), false, false); A = __uint_as_float(r_[0]); B = __uint_as_float(r_[1]); } while (0)
__device__ __forceinline__ int krow_inv(int rho) {
    return (rho & 19) | ((rho & 4) << 1) | ((rho & 8) >> 1);
}
#define NF 0
__device__ __forceinline__ int kstage(int s, int koff) {
    if (s < NF) return s;
    int t = s - NF + koff; t = t >= 10 - NF ? t - (10 - NF) : t;
    return NF + t;
}
constexpr float LOG2E = 1.4426950408889634f;

__device__ __forceinline__ void feat_map(const f32x16& p, unsigned (&o)[10]) {
    float mx = fmaxf(fmaxf(p[0], p[1]), p[2]), mn = fminf(fminf(p[0], p[1]), p[2]);
    mx = fmaxf(fmaxf(mx, p[3]), p[4]); mn = fminf(fminf(mn, p[3]), p[4]);
    mx = fmaxf(fmaxf(mx, p[5]), p[6]); mn = fminf(fminf(mn, p[5]), p[6]);
    mx = fmaxf(fmaxf(mx, p[7]), p[8]); mn = fminf(fminf(mn, p[7]), p[8]);
    mx = fmaxf(mx, p[9]); mn = fminf(mn, p[9]);
    float a, b;
    XHALF(mx, a, b); mx = fmaxf(a, b);
    XHALF(mn, a, b); mn = fminf(a, b);
    const float mxl = -mx * LOG2E, mnl = mn * LOG2E;
    float ep[10], en[10];
#pragma unroll
    for (int i = 0; i < 10; ++i) {
        ep[i] = __builtin_amdgcn_exp2f(fmaf(p[i], LOG2E, mxl));
        en[i] = __builtin_amdgcn_exp2f(fmaf(p[i], -LOG2E, mnl));
    }
    float sp = ((ep[0] + ep[1]) + (ep[2] + ep[3])) + ((ep[4] + ep[5]) + (ep[6] + ep[7])) + (ep[8] + ep[9]);
    float sn = ((en[0] + en[1]) + (en[2] + en[3])) + ((en[4] + en[5]) + (en[6] + en[7])) + (en[8] + en[9]);
    XHALF(sp, a, b); sp = a + b;
    XHALF(sn, a, b); sn = a + b;
    const float rp = __builtin_amdgcn_rcpf(sp), rn = __builtin_amdgcn_rcpf(sn);
#pragma unroll
    for (int i = 0; i < 5; ++i) {
        o[i] = pk2(ep[2 * i] * rp, ep[2 * i + 1] * rp);
        o[5 + i] = pk2(en[2 * i] * rn, en[2 * i + 1] * rn);
    }
}

__device__ __forceinline__ void pg_set(ldsp p, unsigned v) { *(volatile LAS unsigned*)p = v; }
__device__ __forceinline__ u32x4 pg_ld(ldsp p) { return *(volatile LAS u32x4*)p; }
__device__ __forceinline__ unsigned pg_min(u32x4 v) { return (unsigned)__builtin_amdgcn_readfirstlane(min(min(v[0], v[1]), min(v[2], v[3]))); }
__device__ __forceinline__ void pg_wait(ldsp p, unsigned need) {
    while (pg_min(pg_ld(p)) < need) __builtin_amdgcn_s_sleep(1);
    asm volatile("" ::: "memory");
}
template <int CW> struct K1Map {
    static constexpr int WM = CW >> 1, SIDE = CW & 1;
    static constexpr int nloc(int i) { return (WM == 1 && i < 5) ? (i + 3) % 5 : i; }
    static constexpr int ntile(int i) { return SIDE ? 14 - nloc(i) : nloc(i); }
    static constexpr int mtile(int a) { return 4 * WM + (SIDE ? ((a + 2) & 3) : a); }
    static constexpr int woff(int i) { return ST_X + (ntile(i) / 5) * 10240 + (ntile(i) % 5) * 32; }
};

template <int CW, bool ISB>
__device__ __forceinline__ void k1_compute(ldsp lds, int lane, int koff, float* gout STAMP_ARG) {
    using M = K1Map<CW>;
    constexpr bool GATE = !ISB && (CW & 1) == 0;
    const int g = lane >> 4, l15 = lane & 15;
    const unsigned xs0 = (unsigned)(l15 * 128 + ((g ^ (lane & 7)) * 16));
    const unsigned wb = (unsigned)((16 * (g >> 1) + 4 * (g & 1) + ((lane >> 2) & 3)) * 160 + (lane & 3) * 8);
    f32x4 acc[8][4], hacc[4];
#pragma unroll
    for (int i = 0; i < 8; ++i)
#pragma unroll
        for (int a = 0; a < 4; ++a) acc[i][a] = f32x4{0.f, 0.f, 0.f, 0.f};
#pragma unroll
    for (int i = 0; i < 4; ++i) hacc[i] = f32x4{0.f, 0.f, 0.f, 0.f};
    unsigned gk = (unsigned)(L_GW + kstage(0, koff) * 128 + g * 16);
    bf16x8 xf0[4], wf0[8], xh0, xf1[4], wf1[8], xh1;
#define RD(XF, WF, XH, BUF, HALF) do { \
        ldsp px = lds + (BUF) + (xs0 ^ ((HALF) * 64)); ldsp pw = lds + (BUF) + wb; \
        _Pragma("unroll") for (int a = 0; a < 4; ++a) XF[a] = lds_b128(px + (16 + 16 * M::mtile(a)) * 128); \
        if (ISB) XH = lds_b128(px); \
        if (GATE) XH = lds_b128(lds + gk + (HALF) * 64); \
        _Pragma("unroll") for (int i = 0; i < 8; ++i) \
            WF[i] = cat2(lds_tr(pw + M::woff(i) + (HALF) * 5120), lds_tr(pw + M::woff(i) + (HALF) * 5120 + 1280)); \
    } while (0)
#define MM(XF, WF, XH) MMR(XF, WF, XH, 0, 8)
#define MMR(XF, WF, XH, I0, I1) do { \
        _Pragma("unroll") for (int i = I0; i < I1; ++i) { \
            _Pragma("unroll") for (int a = 0; a < 4; ++a) { \
                if (i < 7 || a < 2) acc[i][a] = MFMA16(WF[i], XF[a], acc[i][a]); } \
            if (ISB && i < 3) hacc[i] = MFMA16(WF[i], XH, hacc[i]); \
            if (GATE && i < 4) hacc[i] = MFMA16(XH, XF[i], hacc[i]); \
        } \
    } while (0)
#define ILV() do { \
        _Pragma("unroll") for (int t_ = 0; t_ < ((ISB || GATE) ? 21 : 20); ++t_) { \
            __builtin_amdgcn_sched_group_barrier(0x008, 1, 0); __builtin_amdgcn_sched_group_barrier(0x100, 1, 0); } \
        __builtin_amdgcn_sched_group_barrier(0x008, 13, 0); \
    } while (0)
    LBAR();
    pg_wait(lds + L_FL, 1u);
    STAMP(2);
    unsigned cur = 0;
    RD(xf0, wf0, xh0, cur, 0);
    SBAR();
#pragma unroll 1
    for (int s = 0; s < 9; ++s) {
        RD(xf1, wf1, xh1, cur, 1);
        MMR(xf0, wf0, xh0, 0, 6);
        ILV();
        SBAR();
        asm volatile("s_waitcnt lgkmcnt(0)" ::: "memory");
        pg_set(lds + L_FL + 16 + 4 * CW, (unsigned)(s + 1));
        const u32x4 fv = pg_ld(lds + L_FL);
        SBAR();
        MMR(xf0, wf0, xh0, 6, 8);
        SBAR();
        cur = (cur == 2 * ST) ? 0u : cur + ST;
        if (GATE) gk = (unsigned)(L_GW + kstage(s + 1, koff) * 128 + g * 16);
        if (pg_min(fv) < (unsigned)(s + 2)) pg_wait(lds + L_FL, (unsigned)(s + 2));
        asm volatile("" ::: "memory");
        RD(xf0, wf0, xh0, cur, 0);
        MM(xf1, wf1, xh1);
        ILV();
        SBAR();
    }
    RD(xf1, wf1, xh1, cur, 1);
    MM(xf0, wf0, xh0);
    ILV();
    SBAR();
    MM(xf1, wf1, xh1);
#undef ILV
#undef RD
#undef MM
#undef MMR
    STAMP(3);
    LBAR();
    ldsp cb = lds + g * 8 + l15 * CSTR;
#pragma unroll
    for (int i = 0; i < 8; ++i) {
#pragma unroll
        for (int a = 0; a < 4; ++a) {
            if (i < 7 || a < 2) {
                u32x2 v = {pk2(acc[i][a][0], acc[i][a][1]), pk2(acc[i][a][2], acc[i][a][3])};
                *(LAS u32x2*)(cb + M::ntile(i) * 32 + (16 + 16 * M::mtile(a)) * CSTR) = v;
            }
        }
        if (ISB && i < 3 - M::WM) {
            u32x2 v = {pk2(hacc[i][0], hacc[i][1]), pk2(hacc[i][2], hacc[i][3])};
            *(LAS u32x2*)(cb + M::ntile(i) * 32) = v;
        }
    }
    if (GATE && gout != nullptr) {
        const float z = rbf(g == 0 ? hacc[0][0] : (g == 1 ? hacc[1][0] : (g == 2 ? hacc[2][0] : hacc[3][0])));
        __hip_atomic_store(gout + 64 * M::WM + lane, rbf(__builtin_amdgcn_rcpf(1.f + __builtin_amdgcn_exp2f(-z * LOG2E))), __ATOMIC_RELAXED, __HIP_MEMORY_SCOPE_AGENT);
    }
}

template <int LW>
__device__ __forceinline__ void k1_loader(ldsp lds, unsigned ldsb, int lane, const char* x, const char* wsrc, bool isB, int r0, int hg, int koff,
                                          const char* gatew, const char* wphi, const char* woA, const char* woB, int b, const __amdgpu_buffer_rsrc_t wrs STAMP_ARG) {
    if (LW < 2) { const unsigned c = 64u * LW + lane; glds16(gatew, (c < 80u ? c : 79u) * 16u, ldsb + L_GW + LW * 1024); }
    const unsigned xo = (unsigned)((lane >> 3) * 1280 + (((lane & 7) ^ (lane >> 3)) * 16));
    const char* xb = x + (size_t)(r0 - 16) * 1280;
    const char* xh = x + (size_t)(r0 >= 16 ? r0 - 16 : 0) * 1280;
    unsigned wo[5];
    if (LW > 0) {
#pragma unroll
        for (int q = 0; q < 5; ++q) {
            const unsigned c = 64u * q + lane;
            const unsigned rho = (c * 205u) >> 11, ch = c - rho * 10u;
            wo[q] = (unsigned)krow_inv((int)rho) * 3840u + ch * 16u;
        }
    }
    const char* wbase = wsrc + hg * 160;
    const int sc0 = isB ? 1280 : 0, sc1 = isB ? 0 : 1280;
    auto issue = [&](int s, unsigned buf) {
        const int ks = kstage(s, koff);
#pragma unroll
        for (int i = 0; i < 12; ++i) {
            const int p = 12 * LW + i;
            if (p < 18) {
                const char* sb = (p < 2 ? xh : xb) + ks * 128 + p * 10240;
                if (p >= 2 || isB) glds16(sb, xo, ldsb + buf + p * 1024);
            } else {
                const int q = p - 18, seg = q / 10, hf = (q % 10) / 5;
                const char* sb = wbase + (size_t)ks * (64 * 3840) + hf * (32 * 3840) + (seg == 0 ? sc0 : (seg == 1 ? sc1 : 2560));
                glds16(sb, wo[q % 5], ldsb + buf + p * 1024);
            }
        }
    };
    unsigned cur = 0;
#pragma unroll 1
    for (int s = 0; s < 10; ++s) {
        if (s >= 3) pg_wait(lds + L_FL + 16, (unsigned)(s - 2));
        issue(s, cur);
        cur = (cur == 2 * ST) ? 0u : cur + ST;
        if (s == 0) asm volatile("s_barrier" ::: "memory");
        else {
            if (LW == 0 && !isB) asm volatile("s_waitcnt vmcnt(10)" ::: "memory"); else asm volatile("s_waitcnt vmcnt(12)" ::: "memory");
            pg_set(lds + L_FL + 4 * LW, (unsigned)s);
        }
        if (s == 3) {
            const unsigned c = 64u * LW + lane;
            glds16(wphi + hg * 3200, (c < 200u ? c : 199u) * 16u, ldsb + L_WPH + LW * 1024);
            const int bb = b < 200 ? b : 199;
            const int kb = bb / 5, ng = bb - kb * 5, st = kb / 5, j = kb - st * 5;
            const char* wo = ((st >> 2) ? woA : woB) + (size_t)(8 * (st & 3)) * 20 * 1280 + ng * 256;
#pragma unroll
            for (int i = 0; i < 2; ++i) {
                const int kk = 8 * LW + 4 * i + (lane >> 4), e = kk & 7;
                const int head = j < 4 ? 2 * j + (LW >> 1) : 2 * LW + (e >> 2);
                const int d = j < 4 ? 8 * (LW & 1) + e : 16 + (e & 3);
                glds16(wo, (unsigned)((head * 20 + d) * 1280 + (lane & 15) * 16), ldsb + L_WO + (2 * LW + i) * 1024);
            }
        }
    }
}

__global__ __launch_bounds__(512, 2) void k1_qkv(const __hip_bfloat16* __restrict__ x, const __hip_bfloat16* __restrict__ wA,
                                                   const __hip_bfloat16* __restrict__ wB, const __hip_bfloat16* __restrict__ wphi,
                                                   const __hip_bfloat16* __restrict__ gatew, const __hip_bfloat16* __restrict__ woA,
                                                   const __hip_bfloat16* __restrict__ woB, char* __restrict__ ws) {
    __shared__ __attribute__((aligned(16))) char smem[K1_LDS_ALL + 256 + 128];
    ldsp lds = (ldsp)smem;
    STAMP_DECL STAMP(0);
    const int tid = threadIdx.x, lane = tid & 63;
    if (tid < 64) {
        unsigned long long pc0; unsigned sv;
        asm volatile("s_getpc_b64 %1\n\ts_mov_b32 %0, m0\n\ts_mov_b32 m0, %3\n\ts_nop 0\n\tglobal_load_lds_dword %2, %1\n\ts_mov_b32 m0, %0"
                     : "=&s"(sv), "=&s"(pc0) : "v"(tid * 128), "s"((unsigned)(uintptr_t)lds + K1_LDS_ALL) : "memory");
    }
    asm volatile("" :: "s"(x), "s"(wA), "s"(wB), "s"(wphi), "s"(gatew), "s"(woA), "s"(woB), "s"(ws));
    const int w = __builtin_amdgcn_readfirstlane(tid >> 6);
    const int b = blockIdx.x, xcd = b & 7, idx = b >> 3;
    const int rt = 8 * (xcd & 1) + (idx & 7);
    const int cg = 4 * (xcd >> 1) + (idx >> 3);
    const bool isB = cg >= 8;
    const int hg = cg & 7;
    const int r0 = rt * 128;
    const int r32 = lane & 31, hh = lane >> 5;
    const __amdgpu_buffer_rsrc_t wrs = WSRSRC(ws);
    const int koff = (3 * (idx >> 3) + (idx & 7)) % (10 - NF);

    if (w < 4) {
        {
            unsigned long long pc; int len;
            asm volatile("s_getpc_b64 %0\n.Lgm_here:\n\ts_mov_b32 %1, .Lgm_k2end - .Lgm_here" : "=s"(pc), "=s"(len));
            const int off = idx * 2048 + w * 512 + (lane & 3) * 128;
            if (lane < 4 && off < len) {
                unsigned sv;
                asm volatile("s_mov_b32 %0, m0\n\ts_mov_b32 m0, %3\n\ts_nop 0\n\tglobal_load_lds_dword %1, %2\n\ts_mov_b32 m0, %0"
                             : "=&s"(sv) : "v"(off), "s"(pc), "s"((unsigned)(uintptr_t)lds + K1_LDS_ALL) : "memory");
            }
        }
        if (tid < 32) *(volatile LAS unsigned*)(lds + L_FL + tid * 4) = 0u;
        float* gout = cg == 0 ? (float*)(ws + WS_G) + r0 : nullptr;
        STAMP(1);
        if (isB) {
            if (w == 0) k1_compute<0, true>(lds, lane, koff, gout STAMP_PASS); else if (w == 1) k1_compute<1, true>(lds, lane, koff, gout STAMP_PASS);
            else if (w == 2) k1_compute<2, true>(lds, lane, koff, gout STAMP_PASS); else k1_compute<3, true>(lds, lane, koff, gout STAMP_PASS);
        } else {
            if (w == 0) k1_compute<0, false>(lds, lane, koff, gout STAMP_PASS); else if (w == 1) k1_compute<1, false>(lds, lane, koff, gout STAMP_PASS);
            else if (w == 2) k1_compute<2, false>(lds, lane, koff, gout STAMP_PASS); else k1_compute<3, false>(lds, lane, koff, gout STAMP_PASS);
        }
    } else {
        const unsigned ldsb = (unsigned)(uintptr_t)lds;
        const char* wsrc = (const char*)(isB ? wB : wA);
        if (w == 4) k1_loader<0>(lds, ldsb, lane, (const char*)x, wsrc, isB, r0, hg, koff, (const char*)gatew, (const char*)wphi, (const char*)woA, (const char*)woB, b, wrs STAMP_PASS);
        else if (w == 5) k1_loader<1>(lds, ldsb, lane, (const char*)x, wsrc, isB, r0, hg, koff, (const char*)gatew, (const char*)wphi, (const char*)woA, (const char*)woB, b, wrs STAMP_PASS);
        else if (w == 6) k1_loader<2>(lds, ldsb, lane, (const char*)x, wsrc, isB, r0, hg, koff, (const char*)gatew, (const char*)wphi, (const char*)woA, (const char*)woB, b, wrs STAMP_PASS);
        else k1_loader<3>(lds, ldsb, lane, (const char*)x, wsrc, isB, r0, hg, koff, (const char*)gatew, (const char*)wphi, (const char*)woA, (const char*)woB, b, wrs STAMP_PASS);
        asm volatile("s_waitcnt vmcnt(0)" ::: "memory");
        pg_set(lds + L_FL + 4 * (w - 4), 10u);
        pg_wait(lds + L_FL, 6u);
        const int lw = w - 4;
        u32x4 wof[2];
#pragma unroll
        for (int i = 0; i < 2; ++i) {
            ldsp pw = lds + L_WO + (8 * (lane >> 4) + ((lane >> 2) & 3)) * 256 + (2 * lw + i) * 32 + (lane & 3) * 8;
            const u32x2 f0 = lds_tr(pw), f1 = lds_tr(pw + 1024);
            wof[i] = u32x4{f0[0], f0[1], f1[0], f1[1]};
        }
        u32x4 t0, t1;
        {
            const int rho = (r32 & 3) + 4 * (r32 >> 3), ho = (r32 >> 2) & 1;
            const bool vr = rho < 10, v1 = vr && hh == 0;
            const LAS unsigned short* wpp = (const LAS unsigned short*)(lds + L_WPH + lw * 800) + (vr ? 10 * ho + rho : 0);
#pragma unroll
            for (int j = 0; j < 4; ++j) { const unsigned v = (unsigned)wpp[(8 * hh + 2 * j) * 20] | ((unsigned)wpp[(8 * hh + 2 * j + 1) * 20] << 16); t0[j] = vr ? v : 0u; }
#pragma unroll
            for (int j = 0; j < 2; ++j) { const unsigned v = (unsigned)wpp[(16 + 2 * j) * 20] | ((unsigned)wpp[(17 + 2 * j) * 20] << 16); t1[j] = v1 ? v : 0u; }
            t1[2] = 0u; t1[3] = 0u;
        }
        STAMP(3);
        {
            const u32x4 sent = {0xffffffffu, 0xffffffffu, 0xffffffffu, 0xffffffffu};
            const unsigned dm = (unsigned)WS_O + ((b >> 3) * 8 + 4 + ((b >> 1) & 3)) * 20480 + (b & 1) * 10240 + lw * 1024 + lane * 16;
            ST128(wrs, dm, sent); ST128(wrs, dm + 4096, sent);
            if (lw < 2) ST128(wrs, dm + 8192, sent);
        }
        if (b < 200) {
            const int kb = b / 5, ng = b - kb * 5, st = kb / 5, j = kb - st * 5;
#pragma unroll
            for (int i = 0; i < 2; ++i) {
                const int nt = 8 * ng + 2 * lw + i, ct = nt / 5, n = nt - ct * 5;
                ST128(wrs, (unsigned)WS_WOP + (((ct * 8 + st) * 5 + j) * 5 + n) * 1024 + lane * 16, wof[i]);
            }
        }
        if (!isB && rt == 0) {
            const unsigned dst = (unsigned)WS_WPF + (4 * hg + lw) * 2048 + lane * 16;
            ST128(wrs, dst, t0);
            ST128(wrs, dst + 1024, t1);
        }
        LBAR();
        *(LAS u32x4*)(lds + L_WO + lw * 1024 + lane * 16) = t0;
        *(LAS u32x2*)(lds + L_WO + 4096 + lw * 512 + lane * 8) = u32x2{t1[0], t1[1]};
    }
    LBAR();
    STAMP(4);
    if (!isB) {
        const int hl = w & 3, rp = w >> 2;
        const int h = 4 * hg + hl;
        const u32x2 z2 = {0u, 0u};
        const bf16x8 wa0 = lds_b128(lds + L_WO + hl * 1024 + lane * 16);
        const bf16x8 wa1 = cat2(lds_b64(lds + L_WO + 4096 + hl * 512 + lane * 8), z2);
        f32x16 p[2];
#pragma unroll
        for (int u = 0; u < 2; ++u) {
            ldsp cp = lds + (16 + 32 * (2 * rp + u) + r32) * CSTR + (80 + 20 * hl) * 2;
            bf16x8 b0 = cat2(lds_b64(cp + hh * 16), lds_b64(cp + hh * 16 + 8));
            u32x2 t = lds_b64(cp + 32);
            if (hh) t = z2;
            p[u] = MFMA32(wa0, b0, (f32x16{}));
            p[u] = MFMA32(wa1, cat2(t, z2), p[u]);
        }
#pragma unroll
        for (int u = 0; u < 2; ++u) {
            ldsp cp = lds + (16 + 32 * (2 * rp + u) + r32) * CSTR + (20 * hl) * 2;
            u32x2 a0 = lds_b64(cp + hh * 16), a1 = lds_b64(cp + hh * 16 + 8), t = lds_b64(cp + 32);
            const unsigned dst = (unsigned)WS_QF + (h * 64 + (rt * 4 + 2 * rp + u)) * 1280;
            ST128(wrs, dst + lane * 16, (u32x4{a0[0], a0[1], a1[0], a1[1]}));
            if (hh == 0) ST64(wrs, dst + 1024 + r32 * 8, t);
        }
#pragma unroll
        for (int u = 0; u < 2; ++u) {
            unsigned o[10];
            feat_map(p[u], o);
            ldsp pp = lds + L_PHIK + (32 * (2 * rp + u) + r32) * PHSTR + hl * 80 + hh * 40;
#pragma unroll
            for (int i = 0; i < 5; ++i) *(LAS u32x2*)(pp + 8 * i) = u32x2{o[2 * i], o[2 * i + 1]};
        }
        LBAR();
        STAMP(5);
        {
            const int nt = w >> 2;
            const int g = lane >> 4, q = (lane >> 2) & 3, pp = lane & 3, l15 = lane & 15;
            const int rowl = 4 * (4 * (g & 1) + q) + 2 * (g >> 1);
            ldsp pa = lds + L_PHIK + rowl * PHSTR + hl * 80 + pp * 8;
            ldsp pv = lds + (16 + rowl) * CSTR + (160 + 20 * hl + 16 * nt + 4 * pp) * 2;
            f32x4 kv[3];
#pragma unroll
            for (int m = 0; m < 3; ++m) kv[m] = f32x4{0.f, 0.f, 0.f, 0.f};
            const bool ones = (nt == 1) && (l15 == 4);
#pragma unroll
            for (int kk = 0; kk < 4; ++kk) {
                u32x2 v0 = lds_tr(pv + kk * 32 * CSTR), v1 = lds_tr(pv + kk * 32 * CSTR + CSTR);
                if (ones) { v0 = u32x2{0x3f803f80u, 0x3f803f80u}; v1 = v0; }
                bf16x8 vb = cat2(v0, v1);
#pragma unroll
                for (int m = 0; m < 3; ++m) {
                    bf16x8 af = cat2(lds_tr(pa + kk * 32 * PHSTR + m * 32), lds_tr(pa + kk * 32 * PHSTR + PHSTR + m * 32));
                    kv[m] = MFMA16(af, vb, kv[m]);
                }
            }
            const int d = 16 * nt + l15;
            const unsigned dst = (unsigned)WS_KVP + ((rt * 32 + h) * 840 + d * 40 + 4 * g) * 4;
            if (d < 21) {
                ST128(wrs, dst, __builtin_bit_cast(u32x4, kv[0]));
                ST128(wrs, dst + 64, __builtin_bit_cast(u32x4, kv[1]));
                if (g < 2) ST128(wrs, dst + 128, __builtin_bit_cast(u32x4, kv[2]));
            }
        }
    } else {
        const int hl = w & 3;
        const int row = 64 * (w >> 2) + lane;
        const int srow = r0 + row;
        ldsp base = lds + (16 + row) * CSTR + 40 * hl;
        unsigned qv[10];
#pragma unroll
        for (int i = 0; i < 5; ++i) { u32x2 t = lds_b64(base + 160 + 8 * i); qv[2 * i] = t[0]; qv[2 * i + 1] = t[1]; }
        float lg[8];
        const float scale = 0.22360679774997896f;
        {
            u32x2 kk[8][5];
#pragma unroll
            for (int j = 0; j < 8; ++j)
#pragma unroll
                for (int i = 0; i < 5; ++i) kk[j][i] = lds_b64(base - 2 * j * CSTR + 8 * i);
            SBAR();
#pragma unroll
            for (int j = 0; j < 8; ++j) {
                float a0 = 0.f, a1 = 0.f;
#pragma unroll
                for (int i = 0; i < 5; ++i) { a0 = dot2(qv[2 * i], kk[j][i][0], a0); a1 = dot2(qv[2 * i + 1], kk[j][i][1], a1); }
                lg[j] = (srow - 2 * j >= 0) ? (a0 + a1) * scale : -1e9f;
            }
        }
        u32x2 vv[8][5];
#pragma unroll
        for (int j = 0; j < 8; ++j)
#pragma unroll
            for (int i = 0; i < 5; ++i) vv[j][i] = lds_b64(base + 320 - 2 * j * CSTR + 8 * i);
        SBAR();
        float mx = fmaxf(fmaxf(lg[0], lg[1]), lg[2]);
        mx = fmaxf(fmaxf(mx, lg[3]), lg[4]); mx = fmaxf(fmaxf(mx, lg[5]), lg[6]); mx = fmaxf(mx, lg[7]);
        const float mxl = -mx * LOG2E;
        float sum = 0.f;
#pragma unroll
        for (int j = 0; j < 8; ++j) { lg[j] = __builtin_amdgcn_exp2f(fmaf(lg[j], LOG2E, mxl)); sum += lg[j]; }
        const float rs = __builtin_amdgcn_rcpf(sum);
        float o[20];
#pragma unroll
        for (int i = 0; i < 20; ++i) o[i] = 0.f;
#pragma unroll
        for (int jp = 0; jp < 4; ++jp) {
            const unsigned pw = pk2(lg[2 * jp] * rs, lg[2 * jp + 1] * rs);
#pragma unroll
            for (int i = 0; i < 5; ++i) {
                const u32x2 ta = vv[2 * jp][i], tb = vv[2 * jp + 1][i];
#pragma unroll
                for (int e = 0; e < 2; ++e) {
                    const unsigned lo = __builtin_amdgcn_perm(tb[e], ta[e], 0x05040100u);
                    const unsigned hi = __builtin_amdgcn_perm(tb[e], ta[e], 0x07060302u);
                    o[4 * i + 2 * e] = dot2(pw, lo, o[4 * i + 2 * e]);
                    o[4 * i + 2 * e + 1] = dot2(pw, hi, o[4 * i + 2 * e + 1]);
                }
            }
        }
        STAMP(5);
        const int hB = 4 * hg + hl;
        const unsigned ob = (unsigned)WS_O + ((srow >> 6) * 8 + (hB >> 3)) * 20480;
        const unsigned dm = ob + (hB & 7) * 2048 + lane * 32;
        ST128(wrs, dm, (u32x4{pk2(o[0], o[1]), pk2(o[2], o[3]), pk2(o[4], o[5]), pk2(o[6], o[7])}));
        ST128(wrs, dm + 16, (u32x4{pk2(o[8], o[9]), pk2(o[10], o[11]), pk2(o[12], o[13]), pk2(o[14], o[15])}));
        ST64(wrs, ob + 16384 + (hB & 7) * 512 + lane * 8, (u32x2{pk2(o[16], o[17]), pk2(o[18], o[19])}));
    }
    STAMP_OUT(0);
}

constexpr int K2_LDS = 8 * 20480;
constexpr int K2_KV = 4 * 20480;
constexpr int K2_FL = K2_KV + 1792;
__device__ __forceinline__ u32x4 bld128(__amdgpu_buffer_rsrc_t rs, unsigned voff, unsigned soff, bool sc1) {
    return sc1 ? __builtin_amdgcn_raw_buffer_load_b128(rs, voff, soff, 16) : __builtin_amdgcn_raw_buffer_load_b128(rs, voff, soff, 0);
}
template <bool SC1>
__device__ __forceinline__ void k2_ldx(__amdgpu_buffer_rsrc_t ro, unsigned xm, unsigned xt, int j, u32x4 (&xf)[4]) {
#pragma unroll
    for (int m = 0; m < 4; ++m) {
        if (j < 4) xf[m] = bld128(ro, xm + m * 512, j * 4096, SC1);
        else {
            const u32x2 t0 = SC1 ? __builtin_amdgcn_raw_buffer_load_b64(ro, xt + m * 128, 16384, 16) : __builtin_amdgcn_raw_buffer_load_b64(ro, xt + m * 128, 16384, 0);
            const u32x2 t1 = SC1 ? __builtin_amdgcn_raw_buffer_load_b64(ro, xt + m * 128 + 512, 16384, 16) : __builtin_amdgcn_raw_buffer_load_b64(ro, xt + m * 128 + 512, 16384, 0);
            xf[m] = u32x4{t0[0], t0[1], t1[0], t1[1]};
        }
    }
}
__device__ __forceinline__ void k2_ldw(__amdgpu_buffer_rsrc_t rw, unsigned lo, int j, u32x4 (&wf)[5]) {
#pragma unroll
    for (int n = 0; n < 5; ++n) wf[n] = bld128(rw, lo + (n & 3) * 1024, j * 5120 + (n >> 2) * 4096, false);
}
__device__ __forceinline__ void k2_mm(const u32x4 (&xf)[4], const u32x4 (&wf)[5], f32x4 (&acc)[4][5]) {
#pragma unroll
    for (int m = 0; m < 4; ++m)
#pragma unroll
        for (int n = 0; n < 5; ++n) acc[m][n] = MFMA16(__builtin_bit_cast(bf16x8, wf[n]), __builtin_bit_cast(bf16x8, xf[m]), acc[m][n]);
}
__device__ __forceinline__ unsigned k2_max(const u32x4 (&xf)[4], unsigned mx) {
#pragma unroll
    for (int m = 0; m < 4; ++m) { mx = max(max(mx, xf[m][0]), xf[m][1]); mx = max(max(mx, xf[m][2]), xf[m][3]); }
    return mx;
}

__global__ __launch_bounds__(512, 2) void k2_out(char* __restrict__ ws, __hip_bfloat16* __restrict__ out) {
    __shared__ __attribute__((aligned(16))) char smem[K2_LDS];
    ldsp lds = (ldsp)smem;
    STAMP_DECL STAMP(0);
    const int tid = threadIdx.x, lane = tid & 63;
    const int w = __builtin_amdgcn_readfirstlane(tid >> 6);
    const int b = blockIdx.x, xcd = b & 7, idx = b >> 3;
    const int rt = 8 * (xcd & 3) + (idx & 7);
    const int ct = 4 * (xcd >> 2) + (idx >> 3);
    const int r0 = rt * 64;
    const int g = lane >> 4, l15 = lane & 15, r32 = lane & 31, hh = lane >> 5;
    const u32x2 z2 = {0u, 0u};
    const __amdgpu_buffer_rsrc_t ro = __builtin_amdgcn_make_buffer_rsrc(ws + WS_O + (size_t)(rt * 8 + w) * 20480, 0, 20480, 0x00020000);
    const __amdgpu_buffer_rsrc_t rw = __builtin_amdgcn_make_buffer_rsrc(ws + WS_WOP + (size_t)(ct * 8 + w) * 25600, 0, 25600, 0x00020000);
    const unsigned xm = (unsigned)((g >> 1) * 2048 + l15 * 32 + (g & 1) * 16);
    const unsigned xt = (unsigned)(2 * g * 512 + l15 * 8);
    const unsigned lo = (unsigned)lane * 16u;
    f32x4 acc[4][5];
#pragma unroll
    for (int m = 0; m < 4; ++m)
#pragma unroll
        for (int n = 0; n < 5; ++n) acc[m][n] = f32x4{0.f, 0.f, 0.f, 0.f};
    float gv[4];

    if (w >= 4) {
        const int pw = w - 4, pt = tid - 256;
        const int h = b & 31, c = b >> 5;
        {
            u32x4 q0[2]; u32x2 q1[2];
#pragma unroll
            for (int u = 0; u < 2; ++u) {
                const char* pq = ws + WS_QF + ((size_t)h * 64 + c * 8 + 2 * pw + u) * 1280;
                q0[u] = *(const u32x4*)(pq + lane * 16);
                q1[u] = *(const u32x2*)(pq + 1024 + r32 * 8);
            }
            const u32x4 wa0 = *(const u32x4*)(ws + WS_WPF + (size_t)h * 2048 + lane * 16);
            const u32x4 wa1 = *(const u32x4*)(ws + WS_WPF + (size_t)h * 2048 + 1024 + lane * 16);
            f32x4 t[16];
            {
                const int tc = pt < 210 ? pt : 209;
                const char* kp = ws + WS_KVP + (size_t)h * 3360 + tc * 16;
#pragma unroll
                for (int r = 0; r < 16; ++r) t[r] = *(const f32x4*)(kp + (size_t)r * (32 * 3360));
            }
            if (pt < 2) *(volatile LAS unsigned*)(lds + K2_FL + 4 * pt) = 0u;
            LBAR();
            unsigned ph[2][10];
#pragma unroll
            for (int u = 0; u < 2; ++u) {
                u32x2 tq = q1[u]; if (hh) tq = z2;
                f32x16 p = MFMA32(__builtin_bit_cast(bf16x8, wa0), __builtin_bit_cast(bf16x8, q0[u]), (f32x16{}));
                p = MFMA32(__builtin_bit_cast(bf16x8, wa1), cat2(tq, z2), p);
                feat_map(p, ph[u]);
            }
            SBAR();
            const f32x4 ks = (((t[0] + t[1]) + (t[2] + t[3])) + ((t[4] + t[5]) + (t[6] + t[7]))) + (((t[8] + t[9]) + (t[10] + t[11])) + ((t[12] + t[13]) + (t[14] + t[15])));
            if (pt < 210) *(LAS u32x2*)(lds + K2_KV + pt * 8) = u32x2{pk2(ks[0], ks[1]), pk2(ks[2], ks[3])};
            asm volatile("s_waitcnt lgkmcnt(0)" ::: "memory");
            if (lane == 0) __hip_atomic_fetch_add((LAS unsigned*)(lds + K2_FL), 1u, __ATOMIC_RELAXED, __HIP_MEMORY_SCOPE_WORKGROUP);
            STAMP(1);
            while ((unsigned)__builtin_amdgcn_readfirstlane(__hip_atomic_load((LAS unsigned*)(lds + K2_FL), __ATOMIC_RELAXED, __HIP_MEMORY_SCOPE_WORKGROUP)) < 4u) __builtin_amdgcn_s_sleep(1);
            asm volatile("" ::: "memory");
            const int rho = (r32 & 3) + 4 * (r32 >> 3), ho = (r32 >> 2) & 1;
            const bool vr = rho <= 10;
            const int d = rho < 8 ? 8 * ho + rho : (rho < 10 ? 16 + 2 * ho + (rho - 8) : 20);
            ldsp ap = lds + K2_KV + d * 80 + hh * 40;
            u32x2 a00 = lds_b64(ap), a01 = lds_b64(ap + 8), a10 = lds_b64(ap + 16), a11 = lds_b64(ap + 24), a20 = lds_b64(ap + 32);
            asm volatile("s_waitcnt lgkmcnt(0)" ::: "memory");
            if (lane == 0) __hip_atomic_fetch_add((LAS unsigned*)(lds + K2_FL + 4), 1u, __ATOMIC_RELAXED, __HIP_MEMORY_SCOPE_WORKGROUP);
            if (!vr) { a00 = z2; a01 = z2; a10 = z2; a11 = z2; a20 = z2; }
            const __amdgpu_buffer_rsrc_t rs = __builtin_amdgcn_make_buffer_rsrc(ws + WS_O + (size_t)((c * 4 + pw) * 8 + 4 + (h >> 3)) * 20480, 0, 20480, 0x00020000);
#pragma unroll
            for (int u = 0; u < 2; ++u) {
                f32x16 p = MFMA32(cat2(a00, a01), cat2(u32x2{ph[u][0], ph[u][1]}, u32x2{ph[u][2], ph[u][3]}), (f32x16{}));
                p = MFMA32(cat2(a10, a11), cat2(u32x2{ph[u][4], ph[u][5]}, u32x2{ph[u][6], ph[u][7]}), p);
                p = MFMA32(cat2(a20, z2), cat2(u32x2{ph[u][8], ph[u][9]}, z2), p);
                const float rd = __builtin_amdgcn_rcpf(p[10] + 1e-6f);
                const int srow = u * 32 + r32;
                u32x4 m = {pk2(p[0] * rd, p[1] * rd), pk2(p[2] * rd, p[3] * rd), pk2(p[4] * rd, p[5] * rd), pk2(p[6] * rd, p[7] * rd)};
                __builtin_amdgcn_raw_buffer_store_b128(m, rs, (unsigned)((h & 7) * 2048 + srow * 32 + hh * 16), 0, 16);
                __builtin_amdgcn_raw_buffer_store_b32(pk2(p[8] * rd, p[9] * rd), rs, (unsigned)(16384 + (h & 7) * 512 + srow * 8 + hh * 4), 0, 16);
            }
            STAMP(2);
        }
        SBAR();
        u32x4 wf[3][5], xf[3][4];
#pragma unroll
        for (int j = 0; j < 3; ++j) k2_ldw(rw, lo, j, wf[j]);
#pragma unroll
        for (int m = 0; m < 4; ++m) gv[m] = ((const float*)(ws + WS_G))[r0 + 16 * m + l15];
        SBAR();
        int redo = 0;
#pragma unroll 1
        for (;;) {
#pragma unroll
            for (int j = 0; j < 3; ++j) k2_ldx<true>(ro, xm, xt, j, xf[j]);
            unsigned mx = 0u;
#pragma unroll
            for (int j = 0; j < 3; ++j) mx = k2_max(xf[j], mx);
            if (__builtin_amdgcn_ballot_w64(mx == 0xffffffffu) == 0ull || ++redo > (1 << 16)) break;
            __builtin_amdgcn_s_sleep(2);
        }
        STAMP(3);
#pragma unroll
        for (int j = 0; j < 3; ++j) k2_mm(xf[j], wf[j], acc);
        if (w == 4) {
            while ((unsigned)__builtin_amdgcn_readfirstlane(__hip_atomic_load((LAS unsigned*)(lds + K2_FL + 4), __ATOMIC_RELAXED, __HIP_MEMORY_SCOPE_WORKGROUP)) < 4u) __builtin_amdgcn_s_sleep(1);
            asm volatile("" ::: "memory");
        }
        STAMP(4);
#pragma unroll
        for (int m = 0; m < 4; ++m)
#pragma unroll
            for (int n = 0; n < 5; ++n) *(LAS f32x4*)(lds + w * 20480 + (m * 5 + n) * 1024 + lo) = acc[m][n] * gv[m];
    } else {
        asm volatile("s_barrier" ::: "memory");
        {
            u32x4 wf[5][5], xf[5][4];
#pragma unroll
            for (int j = 0; j < 3; ++j) { k2_ldx<false>(ro, xm, xt, j, xf[j]); k2_ldw(rw, lo, j, wf[j]); }
#pragma unroll
            for (int m = 0; m < 4; ++m) gv[m] = ((const float*)(ws + WS_G))[r0 + 16 * m + l15];
            SBAR();
            k2_mm(xf[0], wf[0], acc);
            STAMP(1);
            SBAR();
            k2_ldx<false>(ro, xm, xt, 3, xf[3]); k2_ldw(rw, lo, 3, wf[3]);
            SBAR();
            k2_mm(xf[1], wf[1], acc);
            SBAR();
            k2_ldx<false>(ro, xm, xt, 4, xf[4]); k2_ldw(rw, lo, 4, wf[4]);
            SBAR();
            k2_mm(xf[2], wf[2], acc);
            k2_mm(xf[3], wf[3], acc);
            k2_mm(xf[4], wf[4], acc);
            STAMP(2);
        }
        SBAR();
        const __amdgpu_buffer_rsrc_t ro2 = __builtin_amdgcn_make_buffer_rsrc(ws + WS_O + (size_t)(rt * 8 + w + 4) * 20480, 0, 20480, 0x00020000);
        const __amdgpu_buffer_rsrc_t rw2 = __builtin_amdgcn_make_buffer_rsrc(ws + WS_WOP + (size_t)(ct * 8 + w + 4) * 25600, 0, 25600, 0x00020000);
        u32x4 wf[2][5], xf[2][4];
        k2_ldw(rw2, lo, 3, wf[0]); k2_ldw(rw2, lo, 4, wf[1]);
        f32x4 accb[4][5];
#pragma unroll
        for (int m = 0; m < 4; ++m) {
            const float gb = rbf(1.f - gv[m]);
#pragma unroll
            for (int n = 0; n < 5; ++n) { accb[m][n] = acc[m][n] * gb; acc[m][n] = f32x4{0.f, 0.f, 0.f, 0.f}; }
        }
        SBAR();
        int redo = 0;
#pragma unroll 1
        for (;;) {
            k2_ldx<true>(ro2, xm, xt, 3, xf[0]); k2_ldx<true>(ro2, xm, xt, 4, xf[1]);
            const unsigned mx = k2_max(xf[1], k2_max(xf[0], 0u));
            if (__builtin_amdgcn_ballot_w64(mx == 0xffffffffu) == 0ull || ++redo > (1 << 16)) break;
            __builtin_amdgcn_s_sleep(2);
        }
        STAMP(3);
        k2_mm(xf[0], wf[0], acc);
        k2_mm(xf[1], wf[1], acc);
        STAMP(4);
#pragma unroll
        for (int m = 0; m < 4; ++m)
#pragma unroll
            for (int n = 0; n < 5; ++n) *(LAS f32x4*)(lds + w * 20480 + (m * 5 + n) * 1024 + lo) = acc[m][n] * gv[m] + accb[m][n];
    }
    LBAR();
    asm volatile(".Lgm_k2end:");
    STAMP(5);
#pragma unroll
    for (int i = 0; i < 3; ++i) {
        const int t = w + 8 * i;
        if (t < 20) {
            ldsp p = lds + t * 1024 + lo;
            const f32x4 y = ((*(const LAS f32x4*)(p) + *(const LAS f32x4*)(p + 20480)) + (*(const LAS f32x4*)(p + 40960) + *(const LAS f32x4*)(p + 61440)))
                          + ((*(const LAS f32x4*)(p + 81920) + *(const LAS f32x4*)(p + 102400)) + (*(const LAS f32x4*)(p + 122880) + *(const LAS f32x4*)(p + 143360)));
            const int m = t / 5, n = t - 5 * m;
            *(u32x2*)((char*)out + (size_t)(r0 + 16 * m + l15) * 1280 + ct * 160 + n * 32 + g * 8) = u32x2{pk2(y[0], y[1]), pk2(y[2], y[3])};
        }
    }
    STAMP_OUT(1);
}

extern "C" void kernel_launch(void* const* d_in, const int* in_sizes, int n_in,
                              void* d_out, int out_size, void* d_ws, size_t ws_size,
                              hipStream_t stream) {
    const __hip_bfloat16* x    = (const __hip_bfloat16*)d_in[0];
    const __hip_bfloat16* wA   = (const __hip_bfloat16*)d_in[1];
    const __hip_bfloat16* wphi = (const __hip_bfloat16*)d_in[2];
    const __hip_bfloat16* woA  = (const __hip_bfloat16*)d_in[3];
    const __hip_bfloat16* wB   = (const __hip_bfloat16*)d_in[5];
    const __hip_bfloat16* woB  = (const __hip_bfloat16*)d_in[6];
    const __hip_bfloat16* gw   = (const __hip_bfloat16*)d_in[7];
    char* ws = (char*)d_ws;
    k1_qkv<<<256, 512, 0, stream>>>(x, wA, wB, wphi, gw, woA, woB, ws);
    k2_out<<<256, 512, 0, stream>>>(ws, (__hip_bfloat16*)d_out);
}
```

```cpp
#include <hip/hip_runtime.h>
#include <hip/hip_bf16.h>
#include <stdint.h>

typedef __bf16 bf16x8 __attribute__((ext_vector_type(8)));
typedef __bf16 bf16x2 __attribute__((ext_vector_type(2)));
typedef short s16x4 __attribute__((ext_vector_type(4)));
typedef float f32x2 __attribute__((ext_vector_type(2)));
typedef float f32x4 __attribute__((ext_vector_type(4)));
typedef float f32x16 __attribute__((ext_vector_type(16)));
typedef unsigned u32x2 __attribute__((ext_vector_type(2)));
typedef unsigned u32x4 __attribute__((ext_vector_type(4)));
#define LAS __attribute__((address_space(3)))
typedef LAS char* ldsp;

constexpr size_t WS_QF  = 0;
constexpr size_t WS_O   = WS_QF + 32ull * 64 * 1280;
constexpr size_t WS_KVP = WS_O + 32ull * 8 * 20480;
constexpr size_t WS_WOP = WS_KVP + 16ull * 32 * 840 * 4;
constexpr size_t WS_WPF = WS_WOP + 64ull * 25600;
constexpr size_t WS_G   = WS_WPF + 32ull * 64 * 32;
constexpr size_t WS_CNT = WS_G + 2048 * 4;

constexpr int ST_X = 144 * 128, ST_W = 3 * 64 * 160, ST = ST_X + ST_W;
constexpr int K1_LDS = 3 * ST;
constexpr int CSTR = 488;
constexpr int PHSTR = 328;
constexpr int L_PHIK = 144 * CSTR;
constexpr int L_GW = K1_LDS;
constexpr int L_WPH = L_GW + 2048;
constexpr int L_WO = L_WPH + 4096;
constexpr int K1_LDS_ALL = L_WO + 8192;
constexpr int L_FL = K1_LDS_ALL + 256;

#define STAMP(i)
#define STAMP_DECL
#define STAMP_OUT(kid)
#define STAMP_ARG
#define STAMP_PASS

#define SBAR() __builtin_amdgcn_sched_barrier(0)
#define VBAR(N) asm volatile("s_waitcnt vmcnt(%0)\n\ts_barrier" :: "i"(N) : "memory")
#define LBAR() asm volatile("s_waitcnt lgkmcnt(0)\n\ts_barrier" ::: "memory")
#define MFMA16(a, b, c) __builtin_amdgcn_mfma_f32_16x16x32_bf16(a, b, c, 0, 0, 0)
#define MFMA32(a, b, c) __builtin_amdgcn_mfma_f32_32x32x16_bf16(a, b, c, 0, 0, 0)

__device__ __forceinline__ void glds16(const char* sbase, unsigned voff, unsigned lds_dst) {
    unsigned sv;
    asm volatile("s_mov_b32 %0, m0\n\ts_mov_b32 m0, %3\n\ts_nop 0\n\tglobal_load_lds_dwordx4 %1, %2\n\ts_mov_b32 m0, %0"
                 : "=&s"(sv) : "v"(voff), "s"(sbase), "s"(lds_dst) : "memory");
}
__device__ __forceinline__ void glds16_sc1(const char* sbase, unsigned voff, unsigned lds_dst) {
    unsigned sv;
    asm volatile("s_mov_b32 %0, m0\n\ts_mov_b32 m0, %3\n\ts_nop 0\n\tglobal_load_lds_dwordx4 %1, %2 sc1\n\ts_mov_b32 m0, %0"
                 : "=&s"(sv) : "v"(voff), "s"(sbase), "s"(lds_dst) : "memory");
}
#define PUBH 6
#define WAE 0
#define EPF 1
#define KST 0
#define CPF 1
#define BDELAY 0
#define STAG 1
#define WPF 1
#define OSC1 4
#define K1AUX 16
#define WSRSRC(ws) __builtin_amdgcn_make_buffer_rsrc((void*)(ws), 0, (int)(WS_CNT + 4096), 0x00020000)
#define ST128(rs, off, v) __builtin_amdgcn_raw_buffer_store_b128((v), rs, (unsigned)(off), 0, K1AUX)
#define ST64(rs, off, v) __builtin_amdgcn_raw_buffer_store_b64((v), rs, (unsigned)(off), 0, K1AUX)
#define ST32(rs, off, v) __builtin_amdgcn_raw_buffer_store_b32((v), rs, (unsigned)(off), 0, K1AUX)
template <int N, bool SC1>
__device__ __forceinline__ void gcopy(const char* sbase, unsigned voff, unsigned lds_dst) {
    unsigned sv;
#define GL_(o) "global_load_lds_dwordx4 %1, %2 offset:" #o
#define GC_(body) asm volatile("s_mov_b32 %0, m0\n\ts_mov_b32 m0, %3\n\ts_nop 0\n\t" body "s_mov_b32 m0, %0" : "=&s"(sv) : "v"(voff), "s"(sbase), "s"(lds_dst) : "memory")
    if constexpr (SC1) {
        if constexpr (N == 4) GC_(GL_(0) " sc1\n\t" GL_(1024) " sc1\n\t" GL_(2048) " sc1\n\t" GL_(3072) " sc1\n\t");
        else if constexpr (N == 2) GC_(GL_(0) " sc1\n\t" GL_(1024) " sc1\n\t");
        else GC_(GL_(0) " sc1\n\t");
    } else {
        if constexpr (N == 4) GC_(GL_(0) "\n\t" GL_(1024) "\n\t" GL_(2048) "\n\t" GL_(3072) "\n\t");
        else if constexpr (N == 2) GC_(GL_(0) "\n\t" GL_(1024) "\n\t");
        else GC_(GL_(0) "\n\t");
    }
#undef GL_
#undef GC_
}
__device__ __forceinline__ bf16x8 lds_b128(ldsp p) { return *(const LAS bf16x8*)p; }
__device__ __forceinline__ u32x2 lds_b64(ldsp p) { return *(const LAS u32x2*)p; }
__device__ __forceinline__ u32x2 lds_tr(ldsp p) {
    return __builtin_bit_cast(u32x2, __builtin_amdgcn_ds_read_tr16_b64_v4i16((LAS s16x4*)p));
}
__device__ __forceinline__ bf16x8 cat2(u32x2 a, u32x2 b) {
    u32x4 r = {a[0], a[1], b[0], b[1]};
    return __builtin_bit_cast(bf16x8, r);
}
__device__ __forceinline__ unsigned pk2(float lo, float hi) {
    bf16x2 r = {(__bf16)lo, (__bf16)hi};
    return __builtin_bit_cast(unsigned, r);
}
__device__ __forceinline__ float rbf(float x) { return (float)(__bf16)x; }
__device__ __forceinline__ float bflo(unsigned u) { return __uint_as_float(u << 16); }
__device__ __forceinline__ float bfhi(unsigned u) { return __uint_as_float(u & 0xffff0000u); }
__device__ __forceinline__ float dot2(unsigned a, unsigned b, float c) {
    return __builtin_amdgcn_fdot2_f32_bf16(__builtin_bit_cast(bf16x2, a), __builtin_bit_cast(bf16x2, b), c, false);
}
#define XHALF(v, A, B) do { auto r_ = __builtin_amdgcn_permlane32_swap(__float_as_uint(v), __float_as_uint(v), false, false); A = __uint_as_float(r_[0]); B = __uint_as_float(r_[1]); } while (0)
__device__ __forceinline__ int krow_inv(int rho) {
    return (rho & 19) | ((rho & 4) << 1) | ((rho & 8) >> 1);
}
#define NF 0
__device__ __forceinline__ int kstage(int s, int koff) {
    if (s < NF) return s;
    int t = s - NF + koff; t = t >= 10 - NF ? t - (10 - NF) : t;
    return NF + t;
}
constexpr float LOG2E = 1.4426950408889634f;

__device__ __forceinline__ void feat_map(const f32x16& p, unsigned (&o)[10]) {
    float mx = fmaxf(fmaxf(p[0], p[1]), p[2]), mn = fminf(fminf(p[0], p[1]), p[2]);
    mx = fmaxf(fmaxf(mx, p[3]), p[4]); mn = fminf(fminf(mn, p[3]), p[4]);
    mx = fmaxf(fmaxf(mx, p[5]), p[6]); mn = fminf(fminf(mn, p[5]), p[6]);
    mx = fmaxf(fmaxf(mx, p[7]), p[8]); mn = fminf(fminf(mn, p[7]), p[8]);
    mx = fmaxf(mx, p[9]); mn = fminf(mn, p[9]);
    float a, b;
    XHALF(mx, a, b); mx = fmaxf(a, b);
    XHALF(mn, a, b); mn = fminf(a, b);
    const f32x2 cp = {LOG2E, LOG2E}, cn = {-LOG2E, -LOG2E};
    const f32x2 mxl = {-mx * LOG2E, -mx * LOG2E}, mnl = {mn * LOG2E, mn * LOG2E};
    f32x2 ep[5], en[5];
#pragma unroll
    for (int i = 0; i < 5; ++i) {
        const f32x2 v = {p[2 * i], p[2 * i + 1]};
        const f32x2 ap = __builtin_elementwise_fma(v, cp, mxl), an = __builtin_elementwise_fma(v, cn, mnl);
        ep[i] = f32x2{__builtin_amdgcn_exp2f(ap[0]), __builtin_amdgcn_exp2f(ap[1])};
        en[i] = f32x2{__builtin_amdgcn_exp2f(an[0]), __builtin_amdgcn_exp2f(an[1])};
    }
    const f32x2 sp2 = ((ep[0] + ep[1]) + (ep[2] + ep[3])) + ep[4], sn2 = ((en[0] + en[1]) + (en[2] + en[3])) + en[4];
    float sp = sp2[0] + sp2[1], sn = sn2[0] + sn2[1];
    XHALF(sp, a, b); sp = a + b;
    XHALF(sn, a, b); sn = a + b;
    const float rp = __builtin_amdgcn_rcpf(sp), rn = __builtin_amdgcn_rcpf(sn);
    const f32x2 rp2 = {rp, rp}, rn2 = {rn, rn};
#pragma unroll
    for (int i = 0; i < 5; ++i) {
        const f32x2 tp = ep[i] * rp2, tn = en[i] * rn2;
        o[i] = pk2(tp[0], tp[1]);
        o[5 + i] = pk2(tn[0], tn[1]);
    }
}

__device__ __forceinline__ void pg_set(ldsp p, unsigned v) { *(volatile LAS unsigned*)p = v; }
__device__ __forceinline__ u32x4 pg_ld(ldsp p) { return *(volatile LAS u32x4*)p; }
__device__ __forceinline__ unsigned pg_min(u32x4 v) { return (unsigned)__builtin_amdgcn_readfirstlane(min(min(v[0], v[1]), min(v[2], v[3]))); }
__device__ __forceinline__ void pg_wait(ldsp p, unsigned need) {
    while (pg_min(pg_ld(p)) < need) __builtin_amdgcn_s_sleep(1);
    asm volatile("" ::: "memory");
}
template <int CW> struct K1Map {
    static constexpr int WM = CW >> 1, SIDE = CW & 1;
    static constexpr int nloc(int i) { return (WM == 1 && i < 5) ? (i + 3) % 5 : i; }
    static constexpr int ntile(int i) { return SIDE ? 14 - nloc(i) : nloc(i); }
    static constexpr int mtile(int a) { return 4 * WM + (SIDE ? ((a + 2) & 3) : a); }
    static constexpr int woff(int i) { return ST_X + (ntile(i) / 5) * 10240 + (ntile(i) % 5) * 32; }
};

template <int CW, bool ISB>
__device__ __forceinline__ void k1_compute(ldsp lds, int lane, int koff, float* gout STAMP_ARG) {
    using M = K1Map<CW>;
    constexpr bool GATE = !ISB && (CW & 1) == 0;
    const int g = lane >> 4, l15 = lane & 15;
    const unsigned xs0 = (unsigned)(l15 * 128 + ((g ^ (lane & 7)) * 16));
    const unsigned wb = (unsigned)((16 * (g >> 1) + 4 * (g & 1) + ((lane >> 2) & 3)) * 160 + (lane & 3) * 8);
    f32x4 acc[8][4], hacc[4];
#pragma unroll
    for (int i = 0; i < 8; ++i)
#pragma unroll
        for (int a = 0; a < 4; ++a) acc[i][a] = f32x4{0.f, 0.f, 0.f, 0.f};
#pragma unroll
    for (int i = 0; i < 4; ++i) hacc[i] = f32x4{0.f, 0.f, 0.f, 0.f};
    unsigned gk = (unsigned)(L_GW + kstage(0, koff) * 128 + g * 16);
    bf16x8 xf0[4], wf0[8], xh0, xf1[4], wf1[8], xh1;
#define RD(XF, WF, XH, BUF, HALF) do { \
        ldsp px = lds + (BUF) + (xs0 ^ ((HALF) * 64)); ldsp pw = lds + (BUF) + wb; \
        _Pragma("unroll") for (int a = 0; a < 4; ++a) XF[a] = lds_b128(px + (16 + 16 * M::mtile(a)) * 128); \
        if (ISB) XH = lds_b128(px); \
        if (GATE) XH = lds_b128(lds + gk + (HALF) * 64); \
        _Pragma("unroll") for (int i = 0; i < 8; ++i) \
            WF[i] = cat2(lds_tr(pw + M::woff(i) + (HALF) * 5120), lds_tr(pw + M::woff(i) + (HALF) * 5120 + 1280)); \
    } while (0)
#define MM(XF, WF, XH) MMR(XF, WF, XH, 0, 8)
#define MMR(XF, WF, XH, I0, I1) do { \
        _Pragma("unroll") for (int i = I0; i < I1; ++i) { \
            _Pragma("unroll") for (int a = 0; a < 4; ++a) { \
                if (i < 7 || a < 2) acc[i][a] = MFMA16(WF[i], XF[a], acc[i][a]); } \
            if (ISB && i < 3) hacc[i] = MFMA16(WF[i], XH, hacc[i]); \
            if (GATE && i < 4) hacc[i] = MFMA16(XH, XF[i], hacc[i]); \
        } \
    } while (0)
#define ILV() do { \
        _Pragma("unroll") for (int t_ = 0; t_ < ((ISB || GATE) ? 21 : 20); ++t_) { \
            __builtin_amdgcn_sched_group_barrier(0x008, 1, 0); __builtin_amdgcn_sched_group_barrier(0x100, 1, 0); } \
        __builtin_amdgcn_sched_group_barrier(0x008, 13, 0); \
    } while (0)
    LBAR();
    pg_wait(lds + L_FL, 1u);
    STAMP(2);
    unsigned cur = 0;
    RD(xf0, wf0, xh0, cur, 0);
    SBAR();
#pragma unroll 1
    for (int s = 0; s < 9; ++s) {
        RD(xf1, wf1, xh1, cur, 1);
        MMR(xf0, wf0, xh0, 0, 6);
        ILV();
        SBAR();
        asm volatile("s_waitcnt lgkmcnt(0)" ::: "memory");
        pg_set(lds + L_FL + 16 + 4 * CW, (unsigned)(s + 1));
        const u32x4 fv = pg_ld(lds + L_FL);
        SBAR();
        MMR(xf0, wf0, xh0, 6, 8);
        SBAR();
        cur = (cur == 2 * ST) ? 0u : cur + ST;
        if (GATE) gk = (unsigned)(L_GW + kstage(s + 1, koff) * 128 + g * 16);
        if (pg_min(fv) < (unsigned)(s + 2)) pg_wait(lds + L_FL, (unsigned)(s + 2));
        asm volatile("" ::: "memory");
        RD(xf0, wf0, xh0, cur, 0);
        MM(xf1, wf1, xh1);
        ILV();
        SBAR();
    }
    RD(xf1, wf1, xh1, cur, 1);
    MM(xf0, wf0, xh0);
    ILV();
    SBAR();
    MM(xf1, wf1, xh1);
#undef ILV
#undef RD
#undef MM
#undef MMR
    STAMP(3);
    LBAR();
    ldsp cb = lds + g * 8 + l15 * CSTR;
#pragma unroll
    for (int i = 0; i < 8; ++i) {
#pragma unroll
        for (int a = 0; a < 4; ++a) {
            if (i < 7 || a < 2) {
                u32x2 v = {pk2(acc[i][a][0], acc[i][a][1]), pk2(acc[i][a][2], acc[i][a][3])};
                *(LAS u32x2*)(cb + M::ntile(i) * 32 + (16 + 16 * M::mtile(a)) * CSTR) = v;
            }
        }
        if (ISB && i < 3 - M::WM) {
            u32x2 v = {pk2(hacc[i][0], hacc[i][1]), pk2(hacc[i][2], hacc[i][3])};
            *(LAS u32x2*)(cb + M::ntile(i) * 32) = v;
        }
    }
    if (GATE && gout != nullptr) {
        const float z = rbf(g == 0 ? hacc[0][0] : (g == 1 ? hacc[1][0] : (g == 2 ? hacc[2][0] : hacc[3][0])));
        __hip_atomic_store(gout + 64 * M::WM + lane, rbf(__builtin_amdgcn_rcpf(1.f + __builtin_amdgcn_exp2f(-z * LOG2E))), __ATOMIC_RELAXED, __HIP_MEMORY_SCOPE_AGENT);
    }
}

template <int V> struct IC { static constexpr int value = V; };
template <int LW>
__device__ __forceinline__ void k1_loader(ldsp lds, unsigned ldsb, int lane, const char* x, const char* wsrc, bool isB, int r0, int hg, int koff,
                                          const char* gatew, const char* wphi, const char* woA, const char* woB, int b, const __amdgpu_buffer_rsrc_t wrs STAMP_ARG) {
    if (LW < 2) { const unsigned c = 64u * LW + lane; glds16(gatew, (c < 80u ? c : 79u) * 16u, ldsb + L_GW + LW * 1024); }
    const unsigned xo = (unsigned)((lane >> 3) * 1280 + (((lane & 7) ^ (lane >> 3)) * 16));
    const char* xb = x + (size_t)(r0 - 16) * 1280;
    const char* xh = x + (size_t)(r0 >= 16 ? r0 - 16 : 0) * 1280;
    unsigned wo[5];
    if (LW > 0) {
#pragma unroll
        for (int q = 0; q < 5; ++q) {
            const unsigned c = 64u * q + lane;
            const unsigned rho = (c * 205u) >> 11, ch = c - rho * 10u;
            wo[q] = (unsigned)krow_inv((int)rho) * 3840u + ch * 16u;
        }
    }
    const char* wbase = wsrc + hg * 160;
    const int sc0 = isB ? 1280 : 0, sc1 = isB ? 0 : 1280;
    auto issue = [&](int s, unsigned buf, auto I0, auto I1) {
        const int ks = kstage(s, koff);
#pragma unroll
        for (int i = I0.value; i < I1.value; ++i) {
            const int p = 12 * LW + i;
            if (p < 18) {
                const char* sb = (p < 2 ? xh : xb) + ks * 128 + p * 10240;
                if (p >= 2 || isB) glds16(sb, xo, ldsb + buf + p * 1024);
            } else {
                const int q = p - 18, seg = q / 10, hf = (q % 10) / 5;
                const char* sb = wbase + (size_t)ks * (64 * 3840) + hf * (32 * 3840) + (seg == 0 ? sc0 : (seg == 1 ? sc1 : 2560));
                glds16(sb, wo[q % 5], ldsb + buf + p * 1024);
            }
        }
    };
    unsigned cur = 0;
#pragma unroll 1
    for (int s = 0; s < 10; ++s) {
        if (s >= 3) pg_wait(lds + L_FL + 16, (unsigned)(s - 2));
        issue(s, cur, IC<0>{}, IC<PUBH>{});
        if (s > 0) {
            if (LW == 0 && !isB) asm volatile("s_waitcnt vmcnt(%0)" :: "i"(PUBH - 2) : "memory"); else asm volatile("s_waitcnt vmcnt(%0)" :: "i"(PUBH) : "memory");
            pg_set(lds + L_FL + 4 * LW, (unsigned)s);
        }
        issue(s, cur, IC<PUBH>{}, IC<12>{});
        cur = (cur == 2 * ST) ? 0u : cur + ST;
        if (s == 0) asm volatile("s_barrier" ::: "memory");
        if (s == 3) {
            const unsigned c = 64u * LW + lane;
            glds16(wphi + hg * 3200, (c < 200u ? c : 199u) * 16u, ldsb + L_WPH + LW * 1024);
            const int bb = b < 200 ? b : 199;
            const int kb = bb / 5, ng = bb - kb * 5, st = kb / 5, j = kb - st * 5;
            const char* wo = ((st >> 2) ? woA : woB) + (size_t)(8 * (st & 3)) * 20 * 1280 + ng * 256;
#pragma unroll
            for (int i = 0; i < 2; ++i) {
                const int kk = 8 * LW + 4 * i + (lane >> 4), e = kk & 7;
                const int head = j < 4 ? 2 * j + (LW >> 1) : 2 * LW + (e >> 2);
                const int d = j < 4 ? 8 * (LW & 1) + e : 16 + (e & 3);
                glds16(wo, (unsigned)((head * 20 + d) * 1280 + (lane & 15) * 16), ldsb + L_WO + (2 * LW + i) * 1024);
            }
        }
    }
}

__global__ __launch_bounds__(512, 2) void k1_qkv(const __hip_bfloat16* __restrict__ x, const __hip_bfloat16* __restrict__ wA,
                                                   const __hip_bfloat16* __restrict__ wB, const __hip_bfloat16* __restrict__ wphi,
                                                   const __hip_bfloat16* __restrict__ gatew, const __hip_bfloat16* __restrict__ woA,
                                                   const __hip_bfloat16* __restrict__ woB, char* __restrict__ ws) {
    __shared__ __attribute__((aligned(16))) char smem[K1_LDS_ALL + 256 + 128];
    ldsp lds = (ldsp)smem;
    STAMP_DECL STAMP(0);
    const int tid = threadIdx.x, lane = tid & 63;
    if (tid < 64) {
        unsigned long long pc0; unsigned sv;
        asm volatile("s_getpc_b64 %1\n\ts_mov_b32 %0, m0\n\ts_mov_b32 m0, %3\n\ts_nop 0\n\tglobal_load_lds_dword %2, %1\n\ts_mov_b32 m0, %0"
                     : "=&s"(sv), "=&s"(pc0) : "v"(tid * 128), "s"((unsigned)(uintptr_t)lds + K1_LDS_ALL) : "memory");
    }
    asm volatile("" :: "s"(x), "s"(wA), "s"(wB), "s"(wphi), "s"(gatew), "s"(woA), "s"(woB), "s"(ws));
    const int w = __builtin_amdgcn_readfirstlane(tid >> 6);
    const int b = blockIdx.x, xcd = b & 7, idx = b >> 3;
    const int rt = 8 * (xcd & 1) + (idx & 7);
    const int cg = 4 * (xcd >> 1) + (idx >> 3);
    const bool isB = cg >= 8;
    const int hg = cg & 7;
    const int r0 = rt * 128;
    const int r32 = lane & 31, hh = lane >> 5;
    const __amdgpu_buffer_rsrc_t wrs = WSRSRC(ws);
    const int koff = (3 * (idx >> 3) + (idx & 7)) % (10 - NF);

    if (w < 4) {
        {
            unsigned long long pc; int len;
            asm volatile("s_getpc_b64 %0\n.Lgm_here:\n\ts_mov_b32 %1, .Lgm_k2end - .Lgm_here" : "=s"(pc), "=s"(len));
            const int off = idx * 2048 + w * 512 + (lane & 3) * 128;
            if (lane < 4 && off < len) {
                unsigned sv;
                asm volatile("s_mov_b32 %0, m0\n\ts_mov_b32 m0, %3\n\ts_nop 0\n\tglobal_load_lds_dword %1, %2\n\ts_mov_b32 m0, %0"
                             : "=&s"(sv) : "v"(off), "s"(pc), "s"((unsigned)(uintptr_t)lds + K1_LDS_ALL) : "memory");
            }
        }
        if (tid < 32) *(volatile LAS unsigned*)(lds + L_FL + tid * 4) = 0u;
        float* gout = cg == 0 ? (float*)(ws + WS_G) + r0 : nullptr;
        STAMP(1);
        if (isB) {
            if (w == 0) k1_compute<0, true>(lds, lane, koff, gout STAMP_PASS); else if (w == 1) k1_compute<1, true>(lds, lane, koff, gout STAMP_PASS);
            else if (w == 2) k1_compute<2, true>(lds, lane, koff, gout STAMP_PASS); else k1_compute<3, true>(lds, lane, koff, gout STAMP_PASS);
        } else {
            if (w == 0) k1_compute<0, false>(lds, lane, koff, gout STAMP_PASS); else if (w == 1) k1_compute<1, false>(lds, lane, koff, gout STAMP_PASS);
            else if (w == 2) k1_compute<2, false>(lds, lane, koff, gout STAMP_PASS); else k1_compute<3, false>(lds, lane, koff, gout STAMP_PASS);
        }
    } else {
        const unsigned ldsb = (unsigned)(uintptr_t)lds;
        const char* wsrc = (const char*)(isB ? wB : wA);
        if (w == 4) k1_loader<0>(lds, ldsb, lane, (const char*)x, wsrc, isB, r0, hg, koff, (const char*)gatew, (const char*)wphi, (const char*)woA, (const char*)woB, b, wrs STAMP_PASS);
        else if (w == 5) k1_loader<1>(lds, ldsb, lane, (const char*)x, wsrc, isB, r0, hg, koff, (const char*)gatew, (const char*)wphi, (const char*)woA, (const char*)woB, b, wrs STAMP_PASS);
        else if (w == 6) k1_loader<2>(lds, ldsb, lane, (const char*)x, wsrc, isB, r0, hg, koff, (const char*)gatew, (const char*)wphi, (const char*)woA, (const char*)woB, b, wrs STAMP_PASS);
        else k1_loader<3>(lds, ldsb, lane, (const char*)x, wsrc, isB, r0, hg, koff, (const char*)gatew, (const char*)wphi, (const char*)woA, (const char*)woB, b, wrs STAMP_PASS);
        asm volatile("s_waitcnt vmcnt(0)" ::: "memory");
        pg_set(lds + L_FL + 4 * (w - 4), 10u);
        pg_wait(lds + L_FL, 6u);
        const int lw = w - 4;
        u32x4 wof[2];
#pragma unroll
        for (int i = 0; i < 2; ++i) {
            ldsp pw = lds + L_WO + (8 * (lane >> 4) + ((lane >> 2) & 3)) * 256 + (2 * lw + i) * 32 + (lane & 3) * 8;
            const u32x2 f0 = lds_tr(pw), f1 = lds_tr(pw + 1024);
            wof[i] = u32x4{f0[0], f0[1], f1[0], f1[1]};
        }
        u32x4 t0, t1;
        {
            const int rho = (r32 & 3) + 4 * (r32 >> 3), ho = (r32 >> 2) & 1;
            const bool vr = rho < 10, v1 = vr && hh == 0;
            const LAS unsigned short* wpp = (const LAS unsigned short*)(lds + L_WPH + lw * 800) + (vr ? 10 * ho + rho : 0);
#pragma unroll
            for (int j = 0; j < 4; ++j) { const unsigned v = (unsigned)wpp[(8 * hh + 2 * j) * 20] | ((unsigned)wpp[(8 * hh + 2 * j + 1) * 20] << 16); t0[j] = vr ? v : 0u; }
#pragma unroll
            for (int j = 0; j < 2; ++j) { const unsigned v = (unsigned)wpp[(16 + 2 * j) * 20] | ((unsigned)wpp[(17 + 2 * j) * 20] << 16); t1[j] = v1 ? v : 0u; }
            t1[2] = 0u; t1[3] = 0u;
        }
        STAMP(3);
        {
            const u32x4 sent = {0xffffffffu, 0xffffffffu, 0xffffffffu, 0xffffffffu};
            const unsigned dm = (unsigned)WS_O + ((b >> 3) * 8 + 4 + ((b >> 1) & 3)) * 20480 + (b & 1) * 10240 + lw * 1024 + lane * 16;
            ST128(wrs, dm, sent); ST128(wrs, dm + 4096, sent);
            if (lw < 2) ST128(wrs, dm + 8192, sent);
        }
        if (b < 200) {
            const int kb = b / 5, ng = b - kb * 5, st = kb / 5, j = kb - st * 5;
#pragma unroll
            for (int i = 0; i < 2; ++i) {
                const int nt = 8 * ng + 2 * lw + i, ct = nt / 5, n = nt - ct * 5;
                ST128(wrs, (unsigned)WS_WOP + (((ct * 8 + st) * 5 + j) * 5 + n) * 1024 + lane * 16, wof[i]);
            }
        }
        if (!isB && rt == 0) {
            const unsigned dst = (unsigned)WS_WPF + (4 * hg + lw) * 2048 + lane * 16;
            ST128(wrs, dst, t0);
            ST128(wrs, dst + 1024, t1);
        }
        LBAR();
        *(LAS u32x4*)(lds + L_WO + lw * 1024 + lane * 16) = t0;
        *(LAS u32x2*)(lds + L_WO + 4096 + lw * 512 + lane * 8) = u32x2{t1[0], t1[1]};
    }
    LBAR();
    STAMP(4);
    if (!isB) {
        const int hl = w & 3, rp = w >> 2;
        const int h = 4 * hg + hl;
        const u32x2 z2 = {0u, 0u};
        const bf16x8 wa0 = lds_b128(lds + L_WO + hl * 1024 + lane * 16);
        const bf16x8 wa1 = cat2(lds_b64(lds + L_WO + 4096 + hl * 512 + lane * 8), z2);
        f32x16 p[2];
#pragma unroll
        for (int u = 0; u < 2; ++u) {
            ldsp cp = lds + (16 + 32 * (2 * rp + u) + r32) * CSTR + (80 + 20 * hl) * 2;
            bf16x8 b0 = cat2(lds_b64(cp + hh * 16), lds_b64(cp + hh * 16 + 8));
            u32x2 t = lds_b64(cp + 32);
            if (hh) t = z2;
            p[u] = MFMA32(wa0, b0, (f32x16{}));
            p[u] = MFMA32(wa1, cat2(t, z2), p[u]);
        }
#pragma unroll
        for (int u = 0; u < 2; ++u) {
            ldsp cp = lds + (16 + 32 * (2 * rp + u) + r32) * CSTR + (20 * hl) * 2;
            u32x2 a0 = lds_b64(cp + hh * 16), a1 = lds_b64(cp + hh * 16 + 8), t = lds_b64(cp + 32);
            const unsigned dst = (unsigned)WS_QF + (h * 64 + (rt * 4 + 2 * rp + u)) * 1280;
            ST128(wrs, dst + lane * 16, (u32x4{a0[0], a0[1], a1[0], a1[1]}));
            if (hh == 0) ST64(wrs, dst + 1024 + r32 * 8, t);
        }
#pragma unroll
        for (int u = 0; u < 2; ++u) {
            unsigned o[10];
            feat_map(p[u], o);
            ldsp pp = lds + L_PHIK + (32 * (2 * rp + u) + r32) * PHSTR + hl * 80 + hh * 40;
#pragma unroll
            for (int i = 0; i < 5; ++i) *(LAS u32x2*)(pp + 8 * i) = u32x2{o[2 * i], o[2 * i + 1]};
        }
        LBAR();
        STAMP(5);
        {
            const int nt = w >> 2;
            const int g = lane >> 4, q = (lane >> 2) & 3, pp = lane & 3, l15 = lane & 15;
            const int rowl = 4 * (4 * (g & 1) + q) + 2 * (g >> 1);
            ldsp pa = lds + L_PHIK + rowl * PHSTR + hl * 80 + pp * 8;
            ldsp pv = lds + (16 + rowl) * CSTR + (160 + 20 * hl + 16 * nt + 4 * pp) * 2;
            f32x4 kv[3];
#pragma unroll
            for (int m = 0; m < 3; ++m) kv[m] = f32x4{0.f, 0.f, 0.f, 0.f};
            const bool ones = (nt == 1) && (l15 == 4);
#pragma unroll
            for (int kk = 0; kk < 4; ++kk) {
                u32x2 v0 = lds_tr(pv + kk * 32 * CSTR), v1 = lds_tr(pv + kk * 32 * CSTR + CSTR);
                if (ones) { v0 = u32x2{0x3f803f80u, 0x3f803f80u}; v1 = v0; }
                bf16x8 vb = cat2(v0, v1);
#pragma unroll
                for (int m = 0; m < 3; ++m) {
                    bf16x8 af = cat2(lds_tr(pa + kk * 32 * PHSTR + m * 32), lds_tr(pa + kk * 32 * PHSTR + PHSTR + m * 32));
                    kv[m] = MFMA16(af, vb, kv[m]);
                }
            }
            const int d = 16 * nt + l15;
            const unsigned dst = (unsigned)WS_KVP + ((rt * 32 + h) * 840 + d * 40 + 4 * g) * 2;
            if (d < 21) {
                ST64(wrs, dst, (u32x2{pk2(kv[0][0], kv[0][1]), pk2(kv[0][2], kv[0][3])}));
                ST64(wrs, dst + 32, (u32x2{pk2(kv[1][0], kv[1][1]), pk2(kv[1][2], kv[1][3])}));
                if (g < 2) ST64(wrs, dst + 64, (u32x2{pk2(kv[2][0], kv[2][1]), pk2(kv[2][2], kv[2][3])}));
            }
        }
    } else {
        const int hl = w & 3;
        const int row = 64 * (w >> 2) + lane;
        const int srow = r0 + row;
        ldsp base = lds + (16 + row) * CSTR + 40 * hl;
        unsigned qv[10];
#pragma unroll
        for (int i = 0; i < 5; ++i) { u32x2 t = lds_b64(base + 160 + 8 * i); qv[2 * i] = t[0]; qv[2 * i + 1] = t[1]; }
        float lg[8];
        const float scale = 0.22360679774997896f;
        {
            u32x2 kk[8][5];
#pragma unroll
            for (int j = 0; j < 8; ++j)
#pragma unroll
                for (int i = 0; i < 5; ++i) kk[j][i] = lds_b64(base - 2 * j * CSTR + 8 * i);
            SBAR();
#pragma unroll
            for (int j = 0; j < 8; ++j) {
                float a0 = 0.f, a1 = 0.f;
#pragma unroll
                for (int i = 0; i < 5; ++i) { a0 = dot2(qv[2 * i], kk[j][i][0], a0); a1 = dot2(qv[2 * i + 1], kk[j][i][1], a1); }
                lg[j] = (srow - 2 * j >= 0) ? (a0 + a1) * scale : -1e9f;
            }
        }
        u32x2 vv[8][5];
#pragma unroll
        for (int j = 0; j < 8; ++j)
#pragma unroll
            for (int i = 0; i < 5; ++i) vv[j][i] = lds_b64(base + 320 - 2 * j * CSTR + 8 * i);
        SBAR();
        float mx = fmaxf(fmaxf(lg[0], lg[1]), lg[2]);
        mx = fmaxf(fmaxf(mx, lg[3]), lg[4]); mx = fmaxf(fmaxf(mx, lg[5]), lg[6]); mx = fmaxf(mx, lg[7]);
        const float mxl = -mx * LOG2E;
        float sum = 0.f;
#pragma unroll
        for (int j = 0; j < 8; ++j) { lg[j] = __builtin_amdgcn_exp2f(fmaf(lg[j], LOG2E, mxl)); sum += lg[j]; }
        const float rs = __builtin_amdgcn_rcpf(sum);
        float o[20];
#pragma unroll
        for (int i = 0; i < 20; ++i) o[i] = 0.f;
#pragma unroll
        for (int jp = 0; jp < 4; ++jp) {
            const unsigned pw = pk2(lg[2 * jp] * rs, lg[2 * jp + 1] * rs);
#pragma unroll
            for (int i = 0; i < 5; ++i) {
                const u32x2 ta = vv[2 * jp][i], tb = vv[2 * jp + 1][i];
#pragma unroll
                for (int e = 0; e < 2; ++e) {
                    const unsigned lo = __builtin_amdgcn_perm(tb[e], ta[e], 0x05040100u);
                    const unsigned hi = __builtin_amdgcn_perm(tb[e], ta[e], 0x07060302u);
                    o[4 * i + 2 * e] = dot2(pw, lo, o[4 * i + 2 * e]);
                    o[4 * i + 2 * e + 1] = dot2(pw, hi, o[4 * i + 2 * e + 1]);
                }
            }
        }
        STAMP(5);
        const int hB = 4 * hg + hl;
        const unsigned ob = (unsigned)WS_O + ((srow >> 6) * 8 + (hB >> 3)) * 20480;
        const unsigned dm = ob + (hB & 7) * 2048 + lane * 32;
        ST128(wrs, dm, (u32x4{pk2(o[0], o[1]), pk2(o[2], o[3]), pk2(o[4], o[5]), pk2(o[6], o[7])}));
        ST128(wrs, dm + 16, (u32x4{pk2(o[8], o[9]), pk2(o[10], o[11]), pk2(o[12], o[13]), pk2(o[14], o[15])}));
        ST64(wrs, ob + 16384 + (hB & 7) * 512 + lane * 8, (u32x2{pk2(o[16], o[17]), pk2(o[18], o[19])}));
    }
    STAMP_OUT(0);
}

constexpr int K2_LDS = 8 * 20480;
constexpr int K2_KV = 4 * 20480;
constexpr int K2_FL = K2_KV + 1792;
__device__ __forceinline__ u32x4 bld128(__amdgpu_buffer_rsrc_t rs, unsigned voff, unsigned soff, bool sc1) {
    return sc1 ? __builtin_amdgcn_raw_buffer_load_b128(rs, voff, soff, 16) : __builtin_amdgcn_raw_buffer_load_b128(rs, voff, soff, 0);
}
template <bool SC1>
__device__ __forceinline__ void k2_ldx(__amdgpu_buffer_rsrc_t ro, unsigned xm, unsigned xt, int j, u32x4 (&xf)[4]) {
#pragma unroll
    for (int m = 0; m < 4; ++m) {
        if (j < 4) xf[m] = bld128(ro, xm + m * 512, j * 4096, SC1);
        else {
            const u32x2 t0 = SC1 ? __builtin_amdgcn_raw_buffer_load_b64(ro, xt + m * 128, 16384, 16) : __builtin_amdgcn_raw_buffer_load_b64(ro, xt + m * 128, 16384, 0);
            const u32x2 t1 = SC1 ? __builtin_amdgcn_raw_buffer_load_b64(ro, xt + m * 128 + 512, 16384, 16) : __builtin_amdgcn_raw_buffer_load_b64(ro, xt + m * 128 + 512, 16384, 0);
            xf[m] = u32x4{t0[0], t0[1], t1[0], t1[1]};
        }
    }
}
__device__ __forceinline__ void k2_ldw(__amdgpu_buffer_rsrc_t rw, unsigned lo, int j, u32x4 (&wf)[5]) {
#pragma unroll
    for (int n = 0; n < 5; ++n) wf[n] = bld128(rw, lo + (n & 3) * 1024, j * 5120 + (n >> 2) * 4096, false);
}
__device__ __forceinline__ void k2_mm(const u32x4 (&xf)[4], const u32x4 (&wf)[5], f32x4 (&acc)[4][5]) {
#pragma unroll
    for (int m = 0; m < 4; ++m)
#pragma unroll
        for (int n = 0; n < 5; ++n) acc[m][n] = MFMA16(__builtin_bit_cast(bf16x8, wf[n]), __builtin_bit_cast(bf16x8, xf[m]), acc[m][n]);
}
__device__ __forceinline__ unsigned k2_max(const u32x4 (&xf)[4], unsigned mx) {
#pragma unroll
    for (int m = 0; m < 4; ++m) { mx = max(max(mx, xf[m][0]), xf[m][1]); mx = max(max(mx, xf[m][2]), xf[m][3]); }
    return mx;
}

__global__ __launch_bounds__(512, 2) void k2_out(char* __restrict__ ws, __hip_bfloat16* __restrict__ out) {
    __shared__ __attribute__((aligned(16))) char smem[K2_LDS];
    ldsp lds = (ldsp)smem;
    STAMP_DECL STAMP(0);
    const int tid = threadIdx.x, lane = tid & 63;
    const int w = __builtin_amdgcn_readfirstlane(tid >> 6);
    const int b = blockIdx.x, xcd = b & 7, idx = b >> 3;
    const int rt = 8 * (xcd & 3) + (idx & 7);
    const int ct = 4 * (xcd >> 2) + (idx >> 3);
    const int r0 = rt * 64;
    const int g = lane >> 4, l15 = lane & 15, r32 = lane & 31, hh = lane >> 5;
    const u32x2 z2 = {0u, 0u};
    const __amdgpu_buffer_rsrc_t ro = __builtin_amdgcn_make_buffer_rsrc(ws + WS_O + (size_t)(rt * 8 + w) * 20480, 0, 20480, 0x00020000);
    const __amdgpu_buffer_rsrc_t rw = __builtin_amdgcn_make_buffer_rsrc(ws + WS_WOP + (size_t)(ct * 8 + w) * 25600, 0, 25600, 0x00020000);
    const unsigned xm = (unsigned)((g >> 1) * 2048 + l15 * 32 + (g & 1) * 16);
    const unsigned xt = (unsigned)(2 * g * 512 + l15 * 8);
    const unsigned lo = (unsigned)lane * 16u;
    f32x4 acc[4][5];
#pragma unroll
    for (int m = 0; m < 4; ++m)
#pragma unroll
        for (int n = 0; n < 5; ++n) acc[m][n] = f32x4{0.f, 0.f, 0.f, 0.f};
    float gv[4];

    if (w >= 4) {
        const int pw = w - 4, pt = tid - 256;
        const int h = b & 31, c = b >> 5;
        u32x4 wf[3][5], xf[3][4];
        {
            u32x4 q0[2]; u32x2 q1[2];
#pragma unroll
            for (int u = 0; u < 2; ++u) {
                const char* pq = ws + WS_QF + ((size_t)h * 64 + c * 8 + 2 * pw + u) * 1280;
                q0[u] = *(const u32x4*)(pq + lane * 16);
                q1[u] = *(const u32x2*)(pq + 1024 + r32 * 8);
            }
            const u32x4 wa0 = *(const u32x4*)(ws + WS_WPF + (size_t)h * 2048 + lane * 16);
            const u32x4 wa1 = *(const u32x4*)(ws + WS_WPF + (size_t)h * 2048 + 1024 + lane * 16);
            u32x2 t[16];
            {
                const int tc = pt < 210 ? pt : 209;
                const char* kp = ws + WS_KVP + (size_t)h * 1680 + tc * 8;
#pragma unroll
                for (int r = 0; r < 16; ++r) t[r] = *(const u32x2*)(kp + (size_t)r * (32 * 1680));
            }
            if (pt < 2) *(volatile LAS unsigned*)(lds + K2_FL + 4 * pt) = 0u;
            LBAR();
            unsigned ph[2][10];
#pragma unroll
            for (int u = 0; u < 2; ++u) {
                u32x2 tq = q1[u]; if (hh) tq = z2;
                f32x16 p = MFMA32(__builtin_bit_cast(bf16x8, wa0), __builtin_bit_cast(bf16x8, q0[u]), (f32x16{}));
                p = MFMA32(__builtin_bit_cast(bf16x8, wa1), cat2(tq, z2), p);
                feat_map(p, ph[u]);
            }
            SBAR();
            f32x4 ks = {0.f, 0.f, 0.f, 0.f};
#pragma unroll
            for (int r = 0; r < 16; ++r) ks += f32x4{bflo(t[r][0]), bfhi(t[r][0]), bflo(t[r][1]), bfhi(t[r][1])};
            if (pt < 210) *(LAS u32x2*)(lds + K2_KV + pt * 8) = u32x2{pk2(ks[0], ks[1]), pk2(ks[2], ks[3])};
            asm volatile("s_waitcnt lgkmcnt(0)" ::: "memory");
            if (lane == 0) __hip_atomic_fetch_add((LAS unsigned*)(lds + K2_FL), 1u, __ATOMIC_RELAXED, __HIP_MEMORY_SCOPE_WORKGROUP);
            STAMP(1);
            while ((unsigned)__builtin_amdgcn_readfirstlane(__hip_atomic_load((LAS unsigned*)(lds + K2_FL), __ATOMIC_RELAXED, __HIP_MEMORY_SCOPE_WORKGROUP)) < 4u) __builtin_amdgcn_s_sleep(1);
            asm volatile("" ::: "memory");
            const int rho = (r32 & 3) + 4 * (r32 >> 3), ho = (r32 >> 2) & 1;
            const bool vr = rho <= 10;
            const int d = rho < 8 ? 8 * ho + rho : (rho < 10 ? 16 + 2 * ho + (rho - 8) : 20);
            ldsp ap = lds + K2_KV + d * 80 + hh * 40;
            u32x2 a00 = lds_b64(ap), a01 = lds_b64(ap + 8), a10 = lds_b64(ap + 16), a11 = lds_b64(ap + 24), a20 = lds_b64(ap + 32);
            asm volatile("s_waitcnt lgkmcnt(0)" ::: "memory");
            if (lane == 0) __hip_atomic_fetch_add((LAS unsigned*)(lds + K2_FL + 4), 1u, __ATOMIC_RELAXED, __HIP_MEMORY_SCOPE_WORKGROUP);
            if (!vr) { a00 = z2; a01 = z2; a10 = z2; a11 = z2; a20 = z2; }
            const __amdgpu_buffer_rsrc_t rs = __builtin_amdgcn_make_buffer_rsrc(ws + WS_O + (size_t)((c * 4 + pw) * 8 + 4 + (h >> 3)) * 20480, 0, 20480, 0x00020000);
#pragma unroll
            for (int u = 0; u < 2; ++u) {
                f32x16 p = MFMA32(cat2(a00, a01), cat2(u32x2{ph[u][0], ph[u][1]}, u32x2{ph[u][2], ph[u][3]}), (f32x16{}));
                p = MFMA32(cat2(a10, a11), cat2(u32x2{ph[u][4], ph[u][5]}, u32x2{ph[u][6], ph[u][7]}), p);
                p = MFMA32(cat2(a20, z2), cat2(u32x2{ph[u][8], ph[u][9]}, z2), p);
                const float rd = __builtin_amdgcn_rcpf(p[10] + 1e-6f);
                const int srow = u * 32 + r32;
                u32x4 m = {pk2(p[0] * rd, p[1] * rd), pk2(p[2] * rd, p[3] * rd), pk2(p[4] * rd, p[5] * rd), pk2(p[6] * rd, p[7] * rd)};
                __builtin_amdgcn_raw_buffer_store_b128(m, rs, (unsigned)((h & 7) * 2048 + srow * 32 + hh * 16), 0, 16);
                __builtin_amdgcn_raw_buffer_store_b32(pk2(p[8] * rd, p[9] * rd), rs, (unsigned)(16384 + (h & 7) * 512 + srow * 8 + hh * 4), 0, 16);
            }
            STAMP(2);
        }
        SBAR();
#pragma unroll
        for (int j = 0; j < 3; ++j) k2_ldw(rw, lo, j, wf[j]);
#pragma unroll
        for (int m = 0; m < 4; ++m) gv[m] = ((const float*)(ws + WS_G))[r0 + 16 * m + l15];
        SBAR();
        int redo = 0;
#pragma unroll 1
        for (;;) {
#pragma unroll
            for (int j = 0; j < 3; ++j) k2_ldx<true>(ro, xm, xt, j, xf[j]);
            unsigned mx = 0u;
#pragma unroll
            for (int j = 0; j < 3; ++j) mx = k2_max(xf[j], mx);
            if (__builtin_amdgcn_ballot_w64(mx == 0xffffffffu) == 0ull || ++redo > (1 << 16)) break;
            __builtin_amdgcn_s_sleep(2);
        }
        STAMP(3);
#pragma unroll
        for (int j = 0; j < 3; ++j) k2_mm(xf[j], wf[j], acc);
        if (w == 4) {
            while ((unsigned)__builtin_amdgcn_readfirstlane(__hip_atomic_load((LAS unsigned*)(lds + K2_FL + 4), __ATOMIC_RELAXED, __HIP_MEMORY_SCOPE_WORKGROUP)) < 4u) __builtin_amdgcn_s_sleep(1);
            asm volatile("" ::: "memory");
        }
        STAMP(4);
#pragma unroll
        for (int m = 0; m < 4; ++m)
#pragma unroll
            for (int n = 0; n < 5; ++n) *(LAS f32x4*)(lds + w * 20480 + (m * 5 + n) * 1024 + lo) = acc[m][n] * gv[m];
    } else {
        asm volatile("s_barrier" ::: "memory");
        {
            u32x4 wf[5][5], xf[5][4];
#pragma unroll
            for (int j = 0; j < 3; ++j) { k2_ldx<false>(ro, xm, xt, j, xf[j]); k2_ldw(rw, lo, j, wf[j]); }
#pragma unroll
            for (int m = 0; m < 4; ++m) gv[m] = ((const float*)(ws + WS_G))[r0 + 16 * m + l15];
            SBAR();
            k2_mm(xf[0], wf[0], acc);
            STAMP(1);
            SBAR();
            k2_ldx<false>(ro, xm, xt, 3, xf[3]); k2_ldw(rw, lo, 3, wf[3]);
            SBAR();
            k2_mm(xf[1], wf[1], acc);
            SBAR();
            k2_ldx<false>(ro, xm, xt, 4, xf[4]); k2_ldw(rw, lo, 4, wf[4]);
            SBAR();
            k2_mm(xf[2], wf[2], acc);
            k2_mm(xf[3], wf[3], acc);
            k2_mm(xf[4], wf[4], acc);
            STAMP(2);
        }
        SBAR();
        const __amdgpu_buffer_rsrc_t ro2 = __builtin_amdgcn_make_buffer_rsrc(ws + WS_O + (size_t)(rt * 8 + w + 4) * 20480, 0, 20480, 0x00020000);
        const __amdgpu_buffer_rsrc_t rw2 = __builtin_amdgcn_make_buffer_rsrc(ws + WS_WOP + (size_t)(ct * 8 + w + 4) * 25600, 0, 25600, 0x00020000);
        u32x4 wf[2][5], xf[2][4];
        k2_ldw(rw2, lo, 3, wf[0]); k2_ldw(rw2, lo, 4, wf[1]);
        f32x4 accb[4][5];
#pragma unroll
        for (int m = 0; m < 4; ++m) {
            const float gb = rbf(1.f - gv[m]);
#pragma unroll
            for (int n = 0; n < 5; ++n) { accb[m][n] = acc[m][n] * gb; acc[m][n] = f32x4{0.f, 0.f, 0.f, 0.f}; }
        }
        SBAR();
        int redo = 0;
#pragma unroll 1
        for (;;) {
            k2_ldx<true>(ro2, xm, xt, 3, xf[0]); k2_ldx<true>(ro2, xm, xt, 4, xf[1]);
            const unsigned mx = k2_max(xf[1], k2_max(xf[0], 0u));
            if (__builtin_amdgcn_ballot_w64(mx == 0xffffffffu) == 0ull || ++redo > (1 << 16)) break;
            __builtin_amdgcn_s_sleep(2);
        }
        STAMP(3);
        k2_mm(xf[0], wf[0], acc);
        k2_mm(xf[1], wf[1], acc);
        STAMP(4);
#pragma unroll
        for (int m = 0; m < 4; ++m)
#pragma unroll
            for (int n = 0; n < 5; ++n) *(LAS f32x4*)(lds + w * 20480 + (m * 5 + n) * 1024 + lo) = acc[m][n] * gv[m] + accb[m][n];
    }
    LBAR();
    asm volatile(".Lgm_k2end:");
    STAMP(5);
#pragma unroll
    for (int i = 0; i < 3; ++i) {
        const int t = w + 8 * i;
        if (t < 20) {
            ldsp p = lds + t * 1024 + lo;
            const f32x4 y = ((*(const LAS f32x4*)(p) + *(const LAS f32x4*)(p + 20480)) + (*(const LAS f32x4*)(p + 40960) + *(const LAS f32x4*)(p + 61440)))
                          + ((*(const LAS f32x4*)(p + 81920) + *(const LAS f32x4*)(p + 102400)) + (*(const LAS f32x4*)(p + 122880) + *(const LAS f32x4*)(p + 143360)));
            const int m = t / 5, n = t - 5 * m;
            *(u32x2*)((char*)out + (size_t)(r0 + 16 * m + l15) * 1280 + ct * 160 + n * 32 + g * 8) = u32x2{pk2(y[0], y[1]), pk2(y[2], y[3])};
        }
    }
    STAMP_OUT(1);
}

extern "C" void kernel_launch(void* const* d_in, const int* in_sizes, int n_in,
                              void* d_out, int out_size, void* d_ws, size_t ws_size,
                              hipStream_t stream) {
    const __hip_bfloat16* x    = (const __hip_bfloat16*)d_in[0];
    const __hip_bfloat16* wA   = (const __hip_bfloat16*)d_in[1];
    const __hip_bfloat16* wphi = (const __hip_bfloat16*)d_in[2];
    const __hip_bfloat16* woA  = (const __hip_bfloat16*)d_in[3];
    const __hip_bfloat16* wB   = (const __hip_bfloat16*)d_in[5];
    const __hip_bfloat16* woB  = (const __hip_bfloat16*)d_in[6];
    const __hip_bfloat16* gw   = (const __hip_bfloat16*)d_in[7];
    char* ws = (char*)d_ws;
    k1_qkv<<<256, 512, 0, stream>>>(x, wA, wB, wphi, gw, woA, woB, ws);
    k2_out<<<256, 512, 0, stream>>>(ws, (__hip_bfloat16*)d_out);
}
```

```cpp
#include <hip/hip_runtime.h>
#include <hip/hip_bf16.h>
#include <stdint.h>

typedef __bf16 bf16x8 __attribute__((ext_vector_type(8)));
typedef __bf16 bf16x2 __attribute__((ext_vector_type(2)));
typedef short s16x4 __attribute__((ext_vector_type(4)));
typedef float f32x2 __attribute__((ext_vector_type(2)));
typedef float f32x4 __attribute__((ext_vector_type(4)));
typedef float f32x16 __attribute__((ext_vector_type(16)));
typedef unsigned u32x2 __attribute__((ext_vector_type(2)));
typedef unsigned u32x4 __attribute__((ext_vector_type(4)));
#define LAS __attribute__((address_space(3)))
typedef LAS char* ldsp;

constexpr size_t WS_QF  = 0;
constexpr size_t WS_O   = WS_QF + 32ull * 64 * 1280;
constexpr size_t WS_KVP = WS_O + 32ull * 8 * 20480;
constexpr size_t WS_WOP = WS_KVP + 16ull * 32 * 840 * 4;
constexpr size_t WS_WPF = WS_WOP + 64ull * 25600;
constexpr size_t WS_G   = WS_WPF + 32ull * 64 * 32;
constexpr size_t WS_CNT = WS_G + 2048 * 4;

constexpr int ST_X = 144 * 128, ST_W = 3 * 64 * 160, ST = ST_X + ST_W;
constexpr int K1_LDS = 3 * ST;
constexpr int CSTR = 592;
constexpr int PHSTR = 328;
constexpr int L_PHIK = 144 * CSTR;
constexpr int L_GW = K1_LDS;
constexpr int L_WPH = L_GW + 2048;
constexpr int L_WO = L_WPH + 4096;
constexpr int K1_LDS_ALL = L_WO + 8192;
constexpr int L_FL = K1_LDS_ALL + 256;

#define STAMP(i)
#define STAMP_DECL
#define STAMP_OUT(kid)
#define STAMP_ARG
#define STAMP_PASS

#define SBAR() __builtin_amdgcn_sched_barrier(0)
#define VBAR(N) asm volatile("s_waitcnt vmcnt(%0)\n\ts_barrier" :: "i"(N) : "memory")
#define LBAR() asm volatile("s_waitcnt lgkmcnt(0)\n\ts_barrier" ::: "memory")
#define MFMA16(a, b, c) __builtin_amdgcn_mfma_f32_16x16x32_bf16(a, b, c, 0, 0, 0)
#define MFMA32(a, b, c) __builtin_amdgcn_mfma_f32_32x32x16_bf16(a, b, c, 0, 0, 0)

__device__ __forceinline__ void glds16(const char* sbase, unsigned voff, unsigned lds_dst) {
    unsigned sv;
    asm volatile("s_mov_b32 %0, m0\n\ts_mov_b32 m0, %3\n\ts_nop 0\n\tglobal_load_lds_dwordx4 %1, %2\n\ts_mov_b32 m0, %0"
                 : "=&s"(sv) : "v"(voff), "s"(sbase), "s"(lds_dst) : "memory");
}
__device__ __forceinline__ void glds16_sc1(const char* sbase, unsigned voff, unsigned lds_dst) {
    unsigned sv;
    asm volatile("s_mov_b32 %0, m0\n\ts_mov_b32 m0, %3\n\ts_nop 0\n\tglobal_load_lds_dwordx4 %1, %2 sc1\n\ts_mov_b32 m0, %0"
                 : "=&s"(sv) : "v"(voff), "s"(sbase), "s"(lds_dst) : "memory");
}
#define PUBH 6
#define WAE 0
#define ILVN 21
#define ILVR 1
#define SPL 6
#define EPF 1
#define KST 0
#define CPF 1
#define BDELAY 0
#define STAG 1
#define WPF 1
#define OSC1 4
#define K1AUX 16
#define WSRSRC(ws) __builtin_amdgcn_make_buffer_rsrc((void*)(ws), 0, (int)(WS_CNT + 4096), 0x00020000)
#define ST128(rs, off, v) __builtin_amdgcn_raw_buffer_store_b128((v), rs, (unsigned)(off), 0, K1AUX)
#define ST64(rs, off, v) __builtin_amdgcn_raw_buffer_store_b64((v), rs, (unsigned)(off), 0, K1AUX)
#define ST32(rs, off, v) __builtin_amdgcn_raw_buffer_store_b32((v), rs, (unsigned)(off), 0, K1AUX)
template <int N, bool SC1>
__device__ __forceinline__ void gcopy(const char* sbase, unsigned voff, unsigned lds_dst) {
    unsigned sv;
#define GL_(o) "global_load_lds_dwordx4 %1, %2 offset:" #o
#define GC_(body) asm volatile("s_mov_b32 %0, m0\n\ts_mov_b32 m0, %3\n\ts_nop 0\n\t" body "s_mov_b32 m0, %0" : "=&s"(sv) : "v"(voff), "s"(sbase), "s"(lds_dst) : "memory")
    if constexpr (SC1) {
        if constexpr (N == 4) GC_(GL_(0) " sc1\n\t" GL_(1024) " sc1\n\t" GL_(2048) " sc1\n\t" GL_(3072) " sc1\n\t");
        else if constexpr (N == 2) GC_(GL_(0) " sc1\n\t" GL_(1024) " sc1\n\t");
        else GC_(GL_(0) " sc1\n\t");
    } else {
        if constexpr (N == 4) GC_(GL_(0) "\n\t" GL_(1024) "\n\t" GL_(2048) "\n\t" GL_(3072) "\n\t");
        else if constexpr (N == 2) GC_(GL_(0) "\n\t" GL_(1024) "\n\t");
        else GC_(GL_(0) "\n\t");
    }
#undef GL_
#undef GC_
}
__device__ __forceinline__ bf16x8 lds_b128(ldsp p) { return *(const LAS bf16x8*)p; }
__device__ __forceinline__ u32x2 lds_b64(ldsp p) { return *(const LAS u32x2*)p; }
__device__ __forceinline__ u32x2 lds_tr(ldsp p) {
    return __builtin_bit_cast(u32x2, __builtin_amdgcn_ds_read_tr16_b64_v4i16((LAS s16x4*)p));
}
__device__ __forceinline__ bf16x8 cat2(u32x2 a, u32x2 b) {
    u32x4 r = {a[0], a[1], b[0], b[1]};
    return __builtin_bit_cast(bf16x8, r);
}
__device__ __forceinline__ unsigned pk2(float lo, float hi) {
    bf16x2 r = {(__bf16)lo, (__bf16)hi};
    return __builtin_bit_cast(unsigned, r);
}
__device__ __forceinline__ float rbf(float x) { return (float)(__bf16)x; }
__device__ __forceinline__ float bflo(unsigned u) { return __uint_as_float(u << 16); }
__device__ __forceinline__ float bfhi(unsigned u) { return __uint_as_float(u & 0xffff0000u); }
__device__ __forceinline__ float dot2(unsigned a, unsigned b, float c) {
    return __builtin_amdgcn_fdot2_f32_bf16(__builtin_bit_cast(bf16x2, a), __builtin_bit_cast(bf16x2, b), c, false);
}
#define XHALF(v, A, B) do { auto r_ = __builtin_amdgcn_permlane32_swap(__float_as_uint(v), __float_as_uint(v), false, false); A = __uint_as_float(r_[0]); B = __uint_as_float(r_[1]); } while (0)
#define XQUAD(v, A, B) do { auto r_ = __builtin_amdgcn_permlane16_swap(__float_as_uint(v), __float_as_uint(v), false, false); A = __uint_as_float(r_[0]); B = __uint_as_float(r_[1]); } while (0)
__device__ __forceinline__ int krow_inv(int rho) {
    return (rho & 19) | ((rho & 4) << 1) | ((rho & 8) >> 1);
}
#define NF 0
__device__ __forceinline__ int kstage(int s, int koff) {
    if (s < NF) return s;
    int t = s - NF + koff; t = t >= 10 - NF ? t - (10 - NF) : t;
    return NF + t;
}
constexpr float LOG2E = 1.4426950408889634f;

__device__ __forceinline__ void feat_map(const f32x16& p, unsigned (&o)[10]) {
    float mx = fmaxf(fmaxf(p[0], p[1]), p[2]), mn = fminf(fminf(p[0], p[1]), p[2]);
    mx = fmaxf(fmaxf(mx, p[3]), p[4]); mn = fminf(fminf(mn, p[3]), p[4]);
    mx = fmaxf(fmaxf(mx, p[5]), p[6]); mn = fminf(fminf(mn, p[5]), p[6]);
    mx = fmaxf(fmaxf(mx, p[7]), p[8]); mn = fminf(fminf(mn, p[7]), p[8]);
    mx = fmaxf(mx, p[9]); mn = fminf(mn, p[9]);
    float a, b;
    XHALF(mx, a, b); mx = fmaxf(a, b);
    XHALF(mn, a, b); mn = fminf(a, b);
    const f32x2 cp = {LOG2E, LOG2E}, cn = {-LOG2E, -LOG2E};
    const f32x2 mxl = {-mx * LOG2E, -mx * LOG2E}, mnl = {mn * LOG2E, mn * LOG2E};
    f32x2 ep[5], en[5];
#pragma unroll
    for (int i = 0; i < 5; ++i) {
        const f32x2 v = {p[2 * i], p[2 * i + 1]};
        const f32x2 ap = __builtin_elementwise_fma(v, cp, mxl), an = __builtin_elementwise_fma(v, cn, mnl);
        ep[i] = f32x2{__builtin_amdgcn_exp2f(ap[0]), __builtin_amdgcn_exp2f(ap[1])};
        en[i] = f32x2{__builtin_amdgcn_exp2f(an[0]), __builtin_amdgcn_exp2f(an[1])};
    }
    const f32x2 sp2 = ((ep[0] + ep[1]) + (ep[2] + ep[3])) + ep[4], sn2 = ((en[0] + en[1]) + (en[2] + en[3])) + en[4];
    float sp = sp2[0] + sp2[1], sn = sn2[0] + sn2[1];
    XHALF(sp, a, b); sp = a + b;
    XHALF(sn, a, b); sn = a + b;
    const float rp = __builtin_amdgcn_rcpf(sp), rn = __builtin_amdgcn_rcpf(sn);
    const f32x2 rp2 = {rp, rp}, rn2 = {rn, rn};
#pragma unroll
    for (int i = 0; i < 5; ++i) {
        const f32x2 tp = ep[i] * rp2, tn = en[i] * rn2;
        o[i] = pk2(tp[0], tp[1]);
        o[5 + i] = pk2(tn[0], tn[1]);
    }
}

__device__ __forceinline__ void pg_set(ldsp p, unsigned v) { *(volatile LAS unsigned*)p = v; }
__device__ __forceinline__ u32x4 pg_ld(ldsp p) { return *(volatile LAS u32x4*)p; }
__device__ __forceinline__ unsigned pg_min(u32x4 v) { return (unsigned)__builtin_amdgcn_readfirstlane(min(min(v[0], v[1]), min(v[2], v[3]))); }
__device__ __forceinline__ void pg_wait(ldsp p, unsigned need) {
    while (pg_min(pg_ld(p)) < need) __builtin_amdgcn_s_sleep(1);
    asm volatile("" ::: "memory");
}
template <int CW> struct K1Map {
    static constexpr int WM = CW >> 1, SIDE = CW & 1;
    static constexpr int nloc(int i) { return (WM == 1 && i < 5) ? (i + 3) % 5 : i; }
    static constexpr int ntile(int i) { return SIDE ? 14 - nloc(i) : nloc(i); }
    static constexpr int mtile(int a) { return 4 * WM + (SIDE ? ((a + 2) & 3) : a); }
    static constexpr int woff(int i) { return ST_X + (ntile(i) / 5) * 10240 + (ntile(i) % 5) * 32; }
};

template <int CW, bool ISB>
__device__ __forceinline__ void k1_compute(ldsp lds, int lane, int koff, float* gout STAMP_ARG) {
    using M = K1Map<CW>;
    constexpr bool GATE = !ISB;
    constexpr int NH = ISB ? 3 - M::WM : 0;
    const int g = lane >> 4, l15 = lane & 15;
    const unsigned xs0 = (unsigned)(l15 * 128 + ((g ^ (lane & 7)) * 16));
    const unsigned wb = (unsigned)((16 * (g >> 1) + 4 * (g & 1) + ((lane >> 2) & 3)) * 160 + (lane & 3) * 8);
    f32x4 acc[8][4], hacc[4];
#pragma unroll
    for (int i = 0; i < 8; ++i)
#pragma unroll
        for (int a = 0; a < 4; ++a) acc[i][a] = f32x4{0.f, 0.f, 0.f, 0.f};
#pragma unroll
    for (int i = 0; i < 4; ++i) hacc[i] = f32x4{0.f, 0.f, 0.f, 0.f};
    unsigned gk = (unsigned)(L_GW + kstage(0, koff) * 128 + g * 16);
    bf16x8 xf0[4], wf0[8], xh0, xf1[4], wf1[8], xh1;
#define RD(XF, WF, XH, BUF, HALF) do { \
        ldsp px = lds + (BUF) + (xs0 ^ ((HALF) * 64)); ldsp pw = lds + (BUF) + wb; \
        _Pragma("unroll") for (int a = 0; a < 4; ++a) XF[a] = lds_b128(px + (16 + 16 * M::mtile(a)) * 128); \
        if (ISB) XH = lds_b128(px); \
        if (GATE) XH = lds_b128(lds + gk + (HALF) * 64); \
        _Pragma("unroll") for (int i = 0; i < 8; ++i) \
            WF[i] = cat2(lds_tr(pw + M::woff(i) + (HALF) * 5120), lds_tr(pw + M::woff(i) + (HALF) * 5120 + 1280)); \
    } while (0)
#define MM(XF, WF, XH) MMR(XF, WF, XH, 0, 8)
#define MMR(XF, WF, XH, I0, I1) do { \
        _Pragma("unroll") for (int i = I0; i < I1; ++i) { \
            _Pragma("unroll") for (int a = 0; a < 4; ++a) { \
                if (i < 7 || a < 2) acc[i][a] = MFMA16(WF[i], XF[a], acc[i][a]); } \
            if (ISB && i < NH) hacc[i] = MFMA16(WF[i], XH, hacc[i]); \
            if (GATE && i < 2) hacc[i] = MFMA16(XH, XF[2 + i], hacc[i]); \
        } \
    } while (0)
#define ILV() do { \
        _Pragma("unroll") for (int t_ = 0; t_ < ILVN; ++t_) { \
            __builtin_amdgcn_sched_group_barrier(0x008, 1, 0); __builtin_amdgcn_sched_group_barrier(0x100, ILVR, 0); } \
        __builtin_amdgcn_sched_group_barrier(0x008, 24, 0); \
    } while (0)
    LBAR();
    pg_wait(lds + L_FL, 1u);
    STAMP(2);
    unsigned cur = 0;
    RD(xf0, wf0, xh0, cur, 0);
    SBAR();
#pragma unroll 1
    for (int s = 0; s < 9; ++s) {
        RD(xf1, wf1, xh1, cur, 1);
        MMR(xf0, wf0, xh0, 0, SPL);
        ILV();
        SBAR();
        asm volatile("s_waitcnt lgkmcnt(0)" ::: "memory");
        pg_set(lds + L_FL + 16 + 4 * CW, (unsigned)(s + 1));
        const u32x4 fv = pg_ld(lds + L_FL);
        SBAR();
        MMR(xf0, wf0, xh0, SPL, 8);
        SBAR();
        cur = (cur == 2 * ST) ? 0u : cur + ST;
        if (GATE) gk = (unsigned)(L_GW + kstage(s + 1, koff) * 128 + g * 16);
        if (pg_min(fv) < (unsigned)(s + 2)) pg_wait(lds + L_FL, (unsigned)(s + 2));
        asm volatile("" ::: "memory");
        RD(xf0, wf0, xh0, cur, 0);
        MM(xf1, wf1, xh1);
        ILV();
        SBAR();
    }
    RD(xf1, wf1, xh1, cur, 1);
    MM(xf0, wf0, xh0);
    ILV();
    SBAR();
    MM(xf1, wf1, xh1);
#undef ILV
#undef RD
#undef MM
#undef MMR
    STAMP(3);
    LBAR();
    ldsp cb = lds + l15 * CSTR;
#pragma unroll
    for (int i = 0; i < 8; ++i) {
        const int cq = 4 * M::ntile(i) + g;
        ldsp cc = cb + 8 * (cq + (cq * 205 >> 10));
#pragma unroll
        for (int a = 0; a < 4; ++a) {
            if (i < 7 || a < 2) {
                u32x2 v = {pk2(acc[i][a][0], acc[i][a][1]), pk2(acc[i][a][2], acc[i][a][3])};
                *(LAS u32x2*)(cc + (16 + 16 * M::mtile(a)) * CSTR) = v;
            }
        }
        if (ISB && i < NH) {
            u32x2 v = {pk2(hacc[i][0], hacc[i][1]), pk2(hacc[i][2], hacc[i][3])};
            *(LAS u32x2*)(cc) = v;
        }
    }
    if (GATE && gout != nullptr) {
        const float z = rbf((g & 1) ? hacc[1][0] : hacc[0][0]);
        if (lane < 32) __hip_atomic_store(gout + 16 * M::mtile(2) + lane, rbf(__builtin_amdgcn_rcpf(1.f + __builtin_amdgcn_exp2f(-z * LOG2E))), __ATOMIC_RELAXED, __HIP_MEMORY_SCOPE_AGENT);
    }
}

template <int V> struct IC { static constexpr int value = V; };
template <int LW>
__device__ __forceinline__ void k1_loader(ldsp lds, unsigned ldsb, int lane, const char* x, const char* wsrc, bool isB, int r0, int hg, int koff,
                                          const char* gatew, const char* wphi, const char* woA, const char* woB, int b, const __amdgpu_buffer_rsrc_t wrs STAMP_ARG) {
    if (LW < 2) { const unsigned c = 64u * LW + lane; glds16(gatew, (c < 80u ? c : 79u) * 16u, ldsb + L_GW + LW * 1024); }
    const unsigned xo = (unsigned)((lane >> 3) * 1280 + (((lane & 7) ^ (lane >> 3)) * 16));
    const char* xb = x + (size_t)(r0 - 16) * 1280;
    const char* xh = x + (size_t)(r0 >= 16 ? r0 - 16 : 0) * 1280;
    unsigned wo[5];
    if (LW > 0) {
#pragma unroll
        for (int q = 0; q < 5; ++q) {
            const unsigned c = 64u * q + lane;
            const unsigned rho = (c * 205u) >> 11, ch = c - rho * 10u;
            wo[q] = (unsigned)krow_inv((int)rho) * 3840u + ch * 16u;
        }
    }
    const char* wbase = wsrc + hg * 160;
    const int sc0 = isB ? 1280 : 0, sc1 = isB ? 0 : 1280;
    auto issue = [&](int s, unsigned buf, auto I0, auto I1) {
        const int ks = kstage(s, koff);
#pragma unroll
        for (int i = I0.value; i < I1.value; ++i) {
            const int p = 12 * LW + i;
            if (p < 18) {
                const char* sb = (p < 2 ? xh : xb) + ks * 128 + p * 10240;
                if (p >= 2 || isB) glds16(sb, xo, ldsb + buf + p * 1024);
            } else {
                const int q = p - 18, seg = q / 10, hf = (q % 10) / 5;
                const char* sb = wbase + (size_t)ks * (64 * 3840) + hf * (32 * 3840) + (seg == 0 ? sc0 : (seg == 1 ? sc1 : 2560));
                glds16(sb, wo[q % 5], ldsb + buf + p * 1024);
            }
        }
    };
    unsigned cur = 0;
#pragma unroll 1
    for (int s = 0; s < 10; ++s) {
        if (s >= 3) pg_wait(lds + L_FL + 16, (unsigned)(s - 2));
        issue(s, cur, IC<0>{}, IC<PUBH>{});
        if (s > 0) {
            if (LW == 0 && !isB) asm volatile("s_waitcnt vmcnt(%0)" :: "i"(PUBH - 2) : "memory"); else asm volatile("s_waitcnt vmcnt(%0)" :: "i"(PUBH) : "memory");
            pg_set(lds + L_FL + 4 * LW, (unsigned)s);
        }
        issue(s, cur, IC<PUBH>{}, IC<12>{});
        cur = (cur == 2 * ST) ? 0u : cur + ST;
        if (s == 0) asm volatile("s_barrier" ::: "memory");
        if (s == 3) {
            const unsigned c = 64u * LW + lane;
            glds16(wphi + hg * 3200, (c < 200u ? c : 199u) * 16u, ldsb + L_WPH + LW * 1024);
            const int bb = b < 200 ? b : 199;
            const int kb = bb / 5, ng = bb - kb * 5, st = kb / 5, j = kb - st * 5;
            const char* wo = ((st >> 2) ? woA : woB) + (size_t)(8 * (st & 3)) * 20 * 1280 + ng * 256;
#pragma unroll
            for (int i = 0; i < 2; ++i) {
                const int kk = 8 * LW + 4 * i + (lane >> 4), e = kk & 7;
                const int head = j < 4 ? 2 * j + (LW >> 1) : 2 * LW + (e >> 2);
                const int d = j < 4 ? 8 * (LW & 1) + e : 16 + (e & 3);
                glds16(wo, (unsigned)((head * 20 + d) * 1280 + (lane & 15) * 16), ldsb + L_WO + (2 * LW + i) * 1024);
            }
        }
    }
}

__global__ __launch_bounds__(512, 2) void k1_qkv(const __hip_bfloat16* __restrict__ x, const __hip_bfloat16* __restrict__ wA,
                                                   const __hip_bfloat16* __restrict__ wB, const __hip_bfloat16* __restrict__ wphi,
                                                   const __hip_bfloat16* __restrict__ gatew, const __hip_bfloat16* __restrict__ woA,
                                                   const __hip_bfloat16* __restrict__ woB, char* __restrict__ ws) {
    __shared__ __attribute__((aligned(16))) char smem[K1_LDS_ALL + 256 + 128];
    ldsp lds = (ldsp)smem;
    STAMP_DECL STAMP(0);
    const int tid = threadIdx.x, lane = tid & 63;
    if (tid < 64) {
        unsigned long long pc0; unsigned sv;
        asm volatile("s_getpc_b64 %1\n\ts_mov_b32 %0, m0\n\ts_mov_b32 m0, %3\n\ts_nop 0\n\tglobal_load_lds_dword %2, %1\n\ts_mov_b32 m0, %0"
                     : "=&s"(sv), "=&s"(pc0) : "v"(tid * 128), "s"((unsigned)(uintptr_t)lds + K1_LDS_ALL) : "memory");
    }
    asm volatile("" :: "s"(x), "s"(wA), "s"(wB), "s"(wphi), "s"(gatew), "s"(woA), "s"(woB), "s"(ws));
    const int w = __builtin_amdgcn_readfirstlane(tid >> 6);
    const int b = blockIdx.x, xcd = b & 7, idx = b >> 3;
    const int rt = 8 * (xcd & 1) + (idx & 7);
    const int cg = 4 * (xcd >> 1) + (idx >> 3);
    const bool isB = cg >= 8;
    const int hg = cg & 7;
    const int r0 = rt * 128;
    const int r32 = lane & 31, hh = lane >> 5;
    const __amdgpu_buffer_rsrc_t wrs = WSRSRC(ws);
    const int koff = (3 * (idx >> 3) + (idx & 7)) % (10 - NF);

    if (w < 4) {
        {
            unsigned long long pc; int len;
            asm volatile("s_getpc_b64 %0\n.Lgm_here:\n\ts_mov_b32 %1, .Lgm_k2end - .Lgm_here" : "=s"(pc), "=s"(len));
            const int off = idx * 2048 + w * 512 + (lane & 3) * 128;
            if (lane < 4 && off < len) {
                unsigned sv;
                asm volatile("s_mov_b32 %0, m0\n\ts_mov_b32 m0, %3\n\ts_nop 0\n\tglobal_load_lds_dword %1, %2\n\ts_mov_b32 m0, %0"
                             : "=&s"(sv) : "v"(off), "s"(pc), "s"((unsigned)(uintptr_t)lds + K1_LDS_ALL) : "memory");
            }
        }
        if (tid < 32) *(volatile LAS unsigned*)(lds + L_FL + tid * 4) = 0u;
        float* gout = cg == 0 ? (float*)(ws + WS_G) + r0 : nullptr;
        STAMP(1);
        if (isB) {
            if (w == 0) k1_compute<0, true>(lds, lane, koff, gout STAMP_PASS); else if (w == 1) k1_compute<1, true>(lds, lane, koff, gout STAMP_PASS);
            else if (w == 2) k1_compute<2, true>(lds, lane, koff, gout STAMP_PASS); else k1_compute<3, true>(lds, lane, koff, gout STAMP_PASS);
        } else {
            if (w == 0) k1_compute<0, false>(lds, lane, koff, gout STAMP_PASS); else if (w == 1) k1_compute<1, false>(lds, lane, koff, gout STAMP_PASS);
            else if (w == 2) k1_compute<2, false>(lds, lane, koff, gout STAMP_PASS); else k1_compute<3, false>(lds, lane, koff, gout STAMP_PASS);
        }
    } else {
        const unsigned ldsb = (unsigned)(uintptr_t)lds;
        const char* wsrc = (const char*)(isB ? wB : wA);
        if (w == 4) k1_loader<0>(lds, ldsb, lane, (const char*)x, wsrc, isB, r0, hg, koff, (const char*)gatew, (const char*)wphi, (const char*)woA, (const char*)woB, b, wrs STAMP_PASS);
        else if (w == 5) k1_loader<1>(lds, ldsb, lane, (const char*)x, wsrc, isB, r0, hg, koff, (const char*)gatew, (const char*)wphi, (const char*)woA, (const char*)woB, b, wrs STAMP_PASS);
        else if (w == 6) k1_loader<2>(lds, ldsb, lane, (const char*)x, wsrc, isB, r0, hg, koff, (const char*)gatew, (const char*)wphi, (const char*)woA, (const char*)woB, b, wrs STAMP_PASS);
        else k1_loader<3>(lds, ldsb, lane, (const char*)x, wsrc, isB, r0, hg, koff, (const char*)gatew, (const char*)wphi, (const char*)woA, (const char*)woB, b, wrs STAMP_PASS);
        asm volatile("s_waitcnt vmcnt(0)" ::: "memory");
        pg_set(lds + L_FL + 4 * (w - 4), 10u);
        pg_wait(lds + L_FL, 6u);
        const int lw = w - 4;
        u32x4 wof[2];
#pragma unroll
        for (int i = 0; i < 2; ++i) {
            ldsp pw = lds + L_WO + (8 * (lane >> 4) + ((lane >> 2) & 3)) * 256 + (2 * lw + i) * 32 + (lane & 3) * 8;
            const u32x2 f0 = lds_tr(pw), f1 = lds_tr(pw + 1024);
            wof[i] = u32x4{f0[0], f0[1], f1[0], f1[1]};
        }
        u32x4 t0, t1;
        {
            const int rho = (r32 & 3) + 4 * (r32 >> 3), ho = (r32 >> 2) & 1;
            const bool vr = rho < 10, v1 = vr && hh == 0;
            const LAS unsigned short* wpp = (const LAS unsigned short*)(lds + L_WPH + lw * 800) + (vr ? 10 * ho + rho : 0);
#pragma unroll
            for (int j = 0; j < 4; ++j) { const unsigned v = (unsigned)wpp[(8 * hh + 2 * j) * 20] | ((unsigned)wpp[(8 * hh + 2 * j + 1) * 20] << 16); t0[j] = vr ? v : 0u; }
#pragma unroll
            for (int j = 0; j < 2; ++j) { const unsigned v = (unsigned)wpp[(16 + 2 * j) * 20] | ((unsigned)wpp[(17 + 2 * j) * 20] << 16); t1[j] = v1 ? v : 0u; }
            t1[2] = 0u; t1[3] = 0u;
        }
        STAMP(3);
        {
            const u32x4 sent = {0xffffffffu, 0xffffffffu, 0xffffffffu, 0xffffffffu};
            const unsigned dm = (unsigned)WS_O + ((b >> 3) * 8 + 4 + ((b >> 1) & 3)) * 20480 + (b & 1) * 10240 + lw * 1024 + lane * 16;
            ST128(wrs, dm, sent); ST128(wrs, dm + 4096, sent);
            if (lw < 2) ST128(wrs, dm + 8192, sent);
        }
        if (b < 200) {
            const int kb = b / 5, ng = b - kb * 5, st = kb / 5, j = kb - st * 5;
#pragma unroll
            for (int i = 0; i < 2; ++i) {
                const int nt = 8 * ng + 2 * lw + i, ct = nt / 5, n = nt - ct * 5;
                ST128(wrs, (unsigned)WS_WOP + (((ct * 8 + st) * 5 + j) * 5 + n) * 1024 + lane * 16, wof[i]);
            }
        }
        if (!isB && rt == 0) {
            const unsigned dst = (unsigned)WS_WPF + (4 * hg + lw) * 2048 + lane * 16;
            ST128(wrs, dst, t0);
            ST128(wrs, dst + 1024, t1);
        }
        LBAR();
        *(LAS u32x4*)(lds + L_WO + lw * 1024 + lane * 16) = t0;
        *(LAS u32x2*)(lds + L_WO + 4096 + lw * 512 + lane * 8) = u32x2{t1[0], t1[1]};
    }
    LBAR();
    STAMP(4);
    if (!isB) {
        const int hl = w & 3, rp = w >> 2;
        const int h = 4 * hg + hl;
        const u32x2 z2 = {0u, 0u};
        const bf16x8 wa0 = lds_b128(lds + L_WO + hl * 1024 + lane * 16);
        const bf16x8 wa1 = cat2(lds_b64(lds + L_WO + 4096 + hl * 512 + lane * 8), z2);
        f32x16 p[2];
#pragma unroll
        for (int u = 0; u < 2; ++u) {
            ldsp cp = lds + (16 + 32 * (2 * rp + u) + r32) * CSTR + 192 + 48 * hl;
            bf16x8 b0 = lds_b128(cp + hh * 16);
            u32x2 t = lds_b64(cp + 32);
            if (hh) t = z2;
            p[u] = MFMA32(wa0, b0, (f32x16{}));
            p[u] = MFMA32(wa1, cat2(t, z2), p[u]);
        }
#pragma unroll
        for (int u = 0; u < 2; ++u) {
            ldsp cp = lds + (16 + 32 * (2 * rp + u) + r32) * CSTR + 48 * hl;
            const u32x4 a0 = *(const LAS u32x4*)(cp + hh * 16); const u32x2 t = lds_b64(cp + 32);
            const unsigned dst = (unsigned)WS_QF + (h * 64 + (rt * 4 + 2 * rp + u)) * 1280;
            ST128(wrs, dst + lane * 16, a0);
            if (hh == 0) ST64(wrs, dst + 1024 + r32 * 8, t);
        }
#pragma unroll
        for (int u = 0; u < 2; ++u) {
            unsigned o[10];
            feat_map(p[u], o);
            ldsp pp = lds + L_PHIK + (32 * (2 * rp + u) + r32) * PHSTR + hl * 80 + hh * 40;
#pragma unroll
            for (int i = 0; i < 5; ++i) *(LAS u32x2*)(pp + 8 * i) = u32x2{o[2 * i], o[2 * i + 1]};
        }
        LBAR();
        STAMP(5);
        {
            const int nt = w >> 2;
            const int g = lane >> 4, q = (lane >> 2) & 3, pp = lane & 3, l15 = lane & 15;
            const int rowl = 4 * (4 * (g & 1) + q) + 2 * (g >> 1);
            ldsp pa = lds + L_PHIK + rowl * PHSTR + hl * 80 + pp * 8;
            ldsp pv = lds + (16 + rowl) * CSTR + 384 + 48 * hl + 32 * nt + 8 * pp;
            f32x4 kv[3];
#pragma unroll
            for (int m = 0; m < 3; ++m) kv[m] = f32x4{0.f, 0.f, 0.f, 0.f};
            const bool ones = (nt == 1) && (l15 == 4);
#pragma unroll
            for (int kk = 0; kk < 4; ++kk) {
                u32x2 v0 = lds_tr(pv + kk * 32 * CSTR), v1 = lds_tr(pv + kk * 32 * CSTR + CSTR);
                if (ones) { v0 = u32x2{0x3f803f80u, 0x3f803f80u}; v1 = v0; }
                bf16x8 vb = cat2(v0, v1);
#pragma unroll
                for (int m = 0; m < 3; ++m) {
                    bf16x8 af = cat2(lds_tr(pa + kk * 32 * PHSTR + m * 32), lds_tr(pa + kk * 32 * PHSTR + PHSTR + m * 32));
                    kv[m] = MFMA16(af, vb, kv[m]);
                }
            }
            const int d = 16 * nt + l15;
            const unsigned dst = (unsigned)WS_KVP + ((rt * 32 + h) * 840 + d * 40 + 4 * g) * 2;
            if (d < 21) {
                ST64(wrs, dst, (u32x2{pk2(kv[0][0], kv[0][1]), pk2(kv[0][2], kv[0][3])}));
                ST64(wrs, dst + 32, (u32x2{pk2(kv[1][0], kv[1][1]), pk2(kv[1][2], kv[1][3])}));
                if (g < 2) ST64(wrs, dst + 64, (u32x2{pk2(kv[2][0], kv[2][1]), pk2(kv[2][2], kv[2][3])}));
            }
        }
    } else {
        const int hl = w & 3, par = w >> 2;
        const int g = lane >> 4, l15 = lane & 15;
        const u32x2 z2 = {0u, 0u};
        const int gc = g < 2 ? g : 2;
        ldsp kq = lds + (16 + 2 * l15 + par) * CSTR + 48 * hl + 16 * gc;
        bf16x8 kf[5], qf[4];
        auto fixk = [&](u32x4 v) { if (g >= 2) { v[2] = 0u; v[3] = 0u; } if (g == 3) { v[0] = 0u; v[1] = 0u; } return __builtin_bit_cast(bf16x8, v); };
        kf[0] = fixk(*(const LAS u32x4*)(lds + (l15 >= 8 ? 2 * l15 - 16 + par : par) * CSTR + 48 * hl + 16 * gc));
#pragma unroll
        for (int kb = 0; kb < 4; ++kb) {
            kf[kb + 1] = fixk(*(const LAS u32x4*)(kq + kb * 32 * CSTR));
            qf[kb] = fixk(*(const LAS u32x4*)(kq + 192 + kb * 32 * CSTR));
        }
        u32x2 vt[5][2];
        {
            const int q4 = (lane >> 2) & 3, pp = lane & 3, key = 4 * g + q4;
            ldsp vp = lds + (16 + 2 * key + par) * CSTR + 384 + 48 * hl + 8 * pp;
            ldsp v0 = lds + (key >= 8 ? 2 * key - 16 + par : par) * CSTR + 384 + 48 * hl + 8 * pp;
            vt[0][0] = lds_tr(v0); vt[0][1] = lds_tr(v0 + 32);
#pragma unroll
            for (int kb = 0; kb < 4; ++kb) { vt[kb + 1][0] = lds_tr(vp + kb * 32 * CSTR); vt[kb + 1][1] = lds_tr(vp + kb * 32 * CSTR + 32); }
        }
        const float NEG = -__builtin_inff();
        float b0[4], b1[4];
#pragma unroll
        for (int e = 0; e < 4; ++e) {
            const int t = 4 * g + e;
            b0[e] = (t >= l15 + 9) ? 0.f : NEG;
            b1[e] = (t <= l15 && t >= l15 - 7) ? 0.f : NEG;
        }
        const float sc = 0.22360679774997896f * LOG2E;
        const int hB = 4 * hg + hl;
        const f32x4 zero4 = {0.f, 0.f, 0.f, 0.f};
        f32x4 s0[4], s1[4];
#pragma unroll
        for (int qb = 0; qb < 4; ++qb) { s0[qb] = MFMA16(kf[qb], qf[qb], zero4); s1[qb] = MFMA16(kf[qb + 1], qf[qb], zero4); }
        float mx[4], sum[4];
#pragma unroll
        for (int qb = 0; qb < 4; ++qb) {
#pragma unroll
            for (int e = 0; e < 4; ++e) {
                s0[qb][e] = fmaf(s0[qb][e], sc, (qb == 0 && rt == 0) ? NEG : b0[e]);
                s1[qb][e] = fmaf(s1[qb][e], sc, b1[e]);
            }
            mx[qb] = fmaxf(fmaxf(fmaxf(s0[qb][0], s0[qb][1]), fmaxf(s0[qb][2], s0[qb][3])), fmaxf(fmaxf(s1[qb][0], s1[qb][1]), fmaxf(s1[qb][2], s1[qb][3])));
        }
#pragma unroll
        for (int qb = 0; qb < 4; ++qb) { float a, bb; XQUAD(mx[qb], a, bb); mx[qb] = fmaxf(a, bb); }
#pragma unroll
        for (int qb = 0; qb < 4; ++qb) { float a, bb; XHALF(mx[qb], a, bb); mx[qb] = fmaxf(a, bb); }
#pragma unroll
        for (int qb = 0; qb < 4; ++qb) {
#pragma unroll
            for (int e = 0; e < 4; ++e) { s0[qb][e] = __builtin_amdgcn_exp2f(s0[qb][e] - mx[qb]); s1[qb][e] = __builtin_amdgcn_exp2f(s1[qb][e] - mx[qb]); }
            const f32x4 t = s0[qb] + s1[qb];
            sum[qb] = (t[0] + t[1]) + (t[2] + t[3]);
        }
#pragma unroll
        for (int qb = 0; qb < 4; ++qb) { float a, bb; XQUAD(sum[qb], a, bb); sum[qb] = a + bb; }
#pragma unroll
        for (int qb = 0; qb < 4; ++qb) { float a, bb; XHALF(sum[qb], a, bb); sum[qb] = __builtin_amdgcn_rcpf(a + bb); }
        u32x2 om[4], ot[4];
#pragma unroll
        for (int qb = 0; qb < 4; ++qb) {
            const f32x4 p0 = s0[qb] * sum[qb], p1 = s1[qb] * sum[qb];
            const bf16x8 pf = cat2(u32x2{pk2(p0[0], p0[1]), pk2(p0[2], p0[3])}, u32x2{pk2(p1[0], p1[1]), pk2(p1[2], p1[3])});
            const f32x4 o0 = MFMA16(cat2(vt[qb][0], vt[qb + 1][0]), pf, zero4);
            const f32x4 o1 = MFMA16(cat2(vt[qb][1], vt[qb + 1][1]), pf, zero4);
            om[qb] = u32x2{pk2(o0[0], o0[1]), pk2(o0[2], o0[3])};
            ot[qb] = u32x2{pk2(o1[0], o1[1]), pk2(o1[2], o1[3])};
        }
        const unsigned ob = (unsigned)WS_O + (2 * rt * 8 + (hB >> 3)) * 20480;
        const unsigned slot = (unsigned)(par * 32 + l15);
#pragma unroll
        for (int qb = 0; qb < 4; ++qb) ST64(wrs, ob + (qb >> 1) * (8 * 20480) + (hB & 7) * 2048 + (slot + 16 * (qb & 1)) * 32 + g * 8, om[qb]);
        if (g == 0) {
#pragma unroll
            for (int qb = 0; qb < 4; ++qb) ST64(wrs, ob + (qb >> 1) * (8 * 20480) + 16384 + (hB & 7) * 512 + (slot + 16 * (qb & 1)) * 8, ot[qb]);
        }
        STAMP(5);
    }
    STAMP_OUT(0);
}

constexpr int K2_LDS = 8 * 20480;
constexpr int K2_KV = 4 * 20480;
constexpr int K2_FL = K2_KV + 1792;
__device__ __forceinline__ u32x4 bld128(__amdgpu_buffer_rsrc_t rs, unsigned voff, unsigned soff, bool sc1) {
    return sc1 ? __builtin_amdgcn_raw_buffer_load_b128(rs, voff, soff, 16) : __builtin_amdgcn_raw_buffer_load_b128(rs, voff, soff, 0);
}
template <bool SC1>
__device__ __forceinline__ void k2_ldx(__amdgpu_buffer_rsrc_t ro, unsigned xm, unsigned xt, int j, u32x4 (&xf)[4]) {
#pragma unroll
    for (int m = 0; m < 4; ++m) {
        if (j < 4) xf[m] = bld128(ro, xm + m * 512, j * 4096, SC1);
        else {
            const u32x2 t0 = SC1 ? __builtin_amdgcn_raw_buffer_load_b64(ro, xt + m * 128, 16384, 16) : __builtin_amdgcn_raw_buffer_load_b64(ro, xt + m * 128, 16384, 0);
            const u32x2 t1 = SC1 ? __builtin_amdgcn_raw_buffer_load_b64(ro, xt + m * 128 + 512, 16384, 16) : __builtin_amdgcn_raw_buffer_load_b64(ro, xt + m * 128 + 512, 16384, 0);
            xf[m] = u32x4{t0[0], t0[1], t1[0], t1[1]};
        }
    }
}
__device__ __forceinline__ void k2_ldw(__amdgpu_buffer_rsrc_t rw, unsigned lo, int j, u32x4 (&wf)[5]) {
#pragma unroll
    for (int n = 0; n < 5; ++n) wf[n] = bld128(rw, lo + (n & 3) * 1024, j * 5120 + (n >> 2) * 4096, false);
}
__device__ __forceinline__ void k2_mm(const u32x4 (&xf)[4], const u32x4 (&wf)[5], f32x4 (&acc)[4][5]) {
#pragma unroll
    for (int m = 0; m < 4; ++m)
#pragma unroll
        for (int n = 0; n < 5; ++n) acc[m][n] = MFMA16(__builtin_bit_cast(bf16x8, wf[n]), __builtin_bit_cast(bf16x8, xf[m]), acc[m][n]);
}
__device__ __forceinline__ unsigned k2_max(const u32x4 (&xf)[4], unsigned mx) {
#pragma unroll
    for (int m = 0; m < 4; ++m) { mx = max(max(mx, xf[m][0]), xf[m][1]); mx = max(max(mx, xf[m][2]), xf[m][3]); }
    return mx;
}

__global__ __launch_bounds__(512, 2) void k2_out(char* __restrict__ ws, __hip_bfloat16* __restrict__ out) {
    __shared__ __attribute__((aligned(16))) char smem[K2_LDS];
    ldsp lds = (ldsp)smem;
    STAMP_DECL STAMP(0);
    const int tid = threadIdx.x, lane = tid & 63;
    const int w = __builtin_amdgcn_readfirstlane(tid >> 6);
    const int b = blockIdx.x, xcd = b & 7, idx = b >> 3;
    const int rt = 8 * (xcd & 3) + (idx & 7);
    const int ct = 4 * (xcd >> 2) + (idx >> 3);
    const int r0 = rt * 64;
    const int g = lane >> 4, l15 = lane & 15, r32 = lane & 31, hh = lane >> 5;
    const u32x2 z2 = {0u, 0u};
    const __amdgpu_buffer_rsrc_t ro = __builtin_amdgcn_make_buffer_rsrc(ws + WS_O + (size_t)(rt * 8 + w) * 20480, 0, 20480, 0x00020000);
    const __amdgpu_buffer_rsrc_t rw = __builtin_amdgcn_make_buffer_rsrc(ws + WS_WOP + (size_t)(ct * 8 + w) * 25600, 0, 25600, 0x00020000);
    const unsigned xm = (unsigned)((g >> 1) * 2048 + l15 * 32 + (g & 1) * 16);
    const unsigned xt = (unsigned)(2 * g * 512 + l15 * 8);
    const unsigned lo = (unsigned)lane * 16u;
    f32x4 acc[4][5];
#pragma unroll
    for (int m = 0; m < 4; ++m)
#pragma unroll
        for (int n = 0; n < 5; ++n) acc[m][n] = f32x4{0.f, 0.f, 0.f, 0.f};
    float gv[4];

    if (w >= 4) {
        const int pw = w - 4, pt = tid - 256;
        const int h = b & 31, c = b >> 5;
        u32x4 wf[3][5], xf[3][4];
        {
            u32x4 q0[2]; u32x2 q1[2];
#pragma unroll
            for (int u = 0; u < 2; ++u) {
                const char* pq = ws + WS_QF + ((size_t)h * 64 + c * 8 + 2 * pw + u) * 1280;
                q0[u] = *(const u32x4*)(pq + lane * 16);
                q1[u] = *(const u32x2*)(pq + 1024 + r32 * 8);
            }
            const u32x4 wa0 = *(const u32x4*)(ws + WS_WPF + (size_t)h * 2048 + lane * 16);
            const u32x4 wa1 = *(const u32x4*)(ws + WS_WPF + (size_t)h * 2048 + 1024 + lane * 16);
            u32x2 t[16];
            {
                const int tc = pt < 210 ? pt : 209;
                const char* kp = ws + WS_KVP + (size_t)h * 1680 + tc * 8;
#pragma unroll
                for (int r = 0; r < 16; ++r) t[r] = *(const u32x2*)(kp + (size_t)r * (32 * 1680));
            }
            if (pt < 2) *(volatile LAS unsigned*)(lds + K2_FL + 4 * pt) = 0u;
            LBAR();
            unsigned ph[2][10];
#pragma unroll
            for (int u = 0; u < 2; ++u) {
                u32x2 tq = q1[u]; if (hh) tq = z2;
                f32x16 p = MFMA32(__builtin_bit_cast(bf16x8, wa0), __builtin_bit_cast(bf16x8, q0[u]), (f32x16{}));
                p = MFMA32(__builtin_bit_cast(bf16x8, wa1), cat2(tq, z2), p);
                feat_map(p, ph[u]);
            }
            SBAR();
            f32x4 ks = {0.f, 0.f, 0.f, 0.f};
#pragma unroll
            for (int r = 0; r < 16; ++r) ks += f32x4{bflo(t[r][0]), bfhi(t[r][0]), bflo(t[r][1]), bfhi(t[r][1])};
            if (pt < 210) *(LAS u32x2*)(lds + K2_KV + pt * 8) = u32x2{pk2(ks[0], ks[1]), pk2(ks[2], ks[3])};
            asm volatile("s_waitcnt lgkmcnt(0)" ::: "memory");
            if (lane == 0) __hip_atomic_fetch_add((LAS unsigned*)(lds + K2_FL), 1u, __ATOMIC_RELAXED, __HIP_MEMORY_SCOPE_WORKGROUP);
            STAMP(1);
            while ((unsigned)__builtin_amdgcn_readfirstlane(__hip_atomic_load((LAS unsigned*)(lds + K2_FL), __ATOMIC_RELAXED, __HIP_MEMORY_SCOPE_WORKGROUP)) < 4u) __builtin_amdgcn_s_sleep(1);
            asm volatile("" ::: "memory");
            const int rho = (r32 & 3) + 4 * (r32 >> 3), ho = (r32 >> 2) & 1;
            const bool vr = rho <= 10;
            const int d = rho < 8 ? 8 * ho + rho : (rho < 10 ? 16 + 2 * ho + (rho - 8) : 20);
            ldsp ap = lds + K2_KV + d * 80 + hh * 40;
            u32x2 a00 = lds_b64(ap), a01 = lds_b64(ap + 8), a10 = lds_b64(ap + 16), a11 = lds_b64(ap + 24), a20 = lds_b64(ap + 32);
            asm volatile("s_waitcnt lgkmcnt(0)" ::: "memory");
            if (lane == 0) __hip_atomic_fetch_add((LAS unsigned*)(lds + K2_FL + 4), 1u, __ATOMIC_RELAXED, __HIP_MEMORY_SCOPE_WORKGROUP);
            if (!vr) { a00 = z2; a01 = z2; a10 = z2; a11 = z2; a20 = z2; }
            const __amdgpu_buffer_rsrc_t rs = __builtin_amdgcn_make_buffer_rsrc(ws + WS_O + (size_t)((c * 4 + pw) * 8 + 4 + (h >> 3)) * 20480, 0, 20480, 0x00020000);
#pragma unroll
            for (int u = 0; u < 2; ++u) {
                f32x16 p = MFMA32(cat2(a00, a01), cat2(u32x2{ph[u][0], ph[u][1]}, u32x2{ph[u][2], ph[u][3]}), (f32x16{}));
                p = MFMA32(cat2(a10, a11), cat2(u32x2{ph[u][4], ph[u][5]}, u32x2{ph[u][6], ph[u][7]}), p);
                p = MFMA32(cat2(a20, z2), cat2(u32x2{ph[u][8], ph[u][9]}, z2), p);
                const float rd = __builtin_amdgcn_rcpf(p[10] + 1e-6f);
                const int srow = (r32 & 1) * 32 + u * 16 + (r32 >> 1);
                u32x4 m = {pk2(p[0] * rd, p[1] * rd), pk2(p[2] * rd, p[3] * rd), pk2(p[4] * rd, p[5] * rd), pk2(p[6] * rd, p[7] * rd)};
                __builtin_amdgcn_raw_buffer_store_b128(m, rs, (unsigned)((h & 7) * 2048 + srow * 32 + hh * 16), 0, 16);
                __builtin_amdgcn_raw_buffer_store_b32(pk2(p[8] * rd, p[9] * rd), rs, (unsigned)(16384 + (h & 7) * 512 + srow * 8 + hh * 4), 0, 16);
            }
            STAMP(2);
        }
        SBAR();
#pragma unroll
        for (int j = 0; j < 3; ++j) k2_ldw(rw, lo, j, wf[j]);
#pragma unroll
        for (int m = 0; m < 4; ++m) gv[m] = ((const float*)(ws + WS_G))[r0 + 32 * (m & 1) + 2 * l15 + (m >> 1)];
        SBAR();
        int redo = 0;
#pragma unroll 1
        for (;;) {
            asm volatile("" ::: "memory");
#pragma unroll
            for (int j = 0; j < 3; ++j) k2_ldx<true>(ro, xm, xt, j, xf[j]);
            unsigned mx = 0u;
#pragma unroll
            for (int j = 0; j < 3; ++j) mx = k2_max(xf[j], mx);
            if (__builtin_amdgcn_ballot_w64(mx == 0xffffffffu) == 0ull || ++redo > (1 << 16)) break;
            __builtin_amdgcn_s_sleep(2);
        }
        STAMP(3);
#pragma unroll
        for (int j = 0; j < 3; ++j) k2_mm(xf[j], wf[j], acc);
        if (w == 4) {
            while ((unsigned)__builtin_amdgcn_readfirstlane(__hip_atomic_load((LAS unsigned*)(lds + K2_FL + 4), __ATOMIC_RELAXED, __HIP_MEMORY_SCOPE_WORKGROUP)) < 4u) __builtin_amdgcn_s_sleep(1);
            asm volatile("" ::: "memory");
        }
        STAMP(4);
#pragma unroll
        for (int m = 0; m < 4; ++m)
#pragma unroll
            for (int n = 0; n < 5; ++n) *(LAS f32x4*)(lds + w * 20480 + (m * 5 + n) * 1024 + lo) = acc[m][n] * gv[m];
    } else {
        asm volatile("s_barrier" ::: "memory");
        {
            u32x4 wf[5][5], xf[5][4];
#pragma unroll
            for (int j = 0; j < 3; ++j) { k2_ldx<false>(ro, xm, xt, j, xf[j]); k2_ldw(rw, lo, j, wf[j]); }
#pragma unroll
            for (int m = 0; m < 4; ++m) gv[m] = ((const float*)(ws + WS_G))[r0 + 32 * (m & 1) + 2 * l15 + (m >> 1)];
            SBAR();
            k2_mm(xf[0], wf[0], acc);
            STAMP(1);
            SBAR();
            k2_ldx<false>(ro, xm, xt, 3, xf[3]); k2_ldw(rw, lo, 3, wf[3]);
            SBAR();
            k2_mm(xf[1], wf[1], acc);
            SBAR();
            k2_ldx<false>(ro, xm, xt, 4, xf[4]); k2_ldw(rw, lo, 4, wf[4]);
            SBAR();
            k2_mm(xf[2], wf[2], acc);
            k2_mm(xf[3], wf[3], acc);
            k2_mm(xf[4], wf[4], acc);
            STAMP(2);
        }
        SBAR();
        const __amdgpu_buffer_rsrc_t ro2 = __builtin_amdgcn_make_buffer_rsrc(ws + WS_O + (size_t)(rt * 8 + w + 4) * 20480, 0, 20480, 0x00020000);
        const __amdgpu_buffer_rsrc_t rw2 = __builtin_amdgcn_make_buffer_rsrc(ws + WS_WOP + (size_t)(ct * 8 + w + 4) * 25600, 0, 25600, 0x00020000);
        u32x4 wf[2][5], xf[2][4];
        k2_ldw(rw2, lo, 3, wf[0]); k2_ldw(rw2, lo, 4, wf[1]);
        f32x4 accb[4][5];
#pragma unroll
        for (int m = 0; m < 4; ++m) {
            const float gb = rbf(1.f - gv[m]);
#pragma unroll
            for (int n = 0; n < 5; ++n) { accb[m][n] = acc[m][n] * gb; acc[m][n] = f32x4{0.f, 0.f, 0.f, 0.f}; }
        }
        SBAR();
        int redo = 0;
#pragma unroll 1
        for (;;) {
            asm volatile("" ::: "memory");
            k2_ldx<true>(ro2, xm, xt, 3, xf[0]); k2_ldx<true>(ro2, xm, xt, 4, xf[1]);
            const unsigned mx = k2_max(xf[1], k2_max(xf[0], 0u));
            if (__builtin_amdgcn_ballot_w64(mx == 0xffffffffu) == 0ull || ++redo > (1 << 16)) break;
            __builtin_amdgcn_s_sleep(2);
        }
        STAMP(3);
        k2_mm(xf[0], wf[0], acc);
        k2_mm(xf[1], wf[1], acc);
        STAMP(4);
#pragma unroll
        for (int m = 0; m < 4; ++m)
#pragma unroll
            for (int n = 0; n < 5; ++n) *(LAS f32x4*)(lds + w * 20480 + (m * 5 + n) * 1024 + lo) = acc[m][n] * gv[m] + accb[m][n];
    }
    LBAR();
    asm volatile(".Lgm_k2end:");
    STAMP(5);
#pragma unroll
    for (int i = 0; i < 3; ++i) {
        const int t = w + 8 * i;
        if (t < 20) {
            ldsp p = lds + t * 1024 + lo;
            const f32x4 y = ((*(const LAS f32x4*)(p) + *(const LAS f32x4*)(p + 20480)) + (*(const LAS f32x4*)(p + 40960) + *(const LAS f32x4*)(p + 61440)))
                          + ((*(const LAS f32x4*)(p + 81920) + *(const LAS f32x4*)(p + 102400)) + (*(const LAS f32x4*)(p + 122880) + *(const LAS f32x4*)(p + 143360)));
            const int m = t / 5, n = t - 5 * m;
            *(u32x2*)((char*)out + (size_t)(r0 + 32 * (m & 1) + 2 * l15 + (m >> 1)) * 1280 + ct * 160 + n * 32 + g * 8) = u32x2{pk2(y[0], y[1]), pk2(y[2], y[3])};
        }
    }
    STAMP_OUT(1);
}

extern "C" void kernel_launch(void* const* d_in, const int* in_sizes, int n_in,
                              void* d_out, int out_size, void* d_ws, size_t ws_size,
                              hipStream_t stream) {
    const __hip_bfloat16* x    = (const __hip_bfloat16*)d_in[0];
    const __hip_bfloat16* wA   = (const __hip_bfloat16*)d_in[1];
    const __hip_bfloat16* wphi = (const __hip_bfloat16*)d_in[2];
    const __hip_bfloat16* woA  = (const __hip_bfloat16*)d_in[3];
    const __hip_bfloat16* wB   = (const __hip_bfloat16*)d_in[5];
    const __hip_bfloat16* woB  = (const __hip_bfloat16*)d_in[6];
    const __hip_bfloat16* gw   = (const __hip_bfloat16*)d_in[7];
    char* ws = (char*)d_ws;
    k1_qkv<<<256, 512, 0, stream>>>(x, wA, wB, wphi, gw, woA, woB, ws);
    k2_out<<<256, 512, 0, stream>>>(ws, (__hip_bfloat16*)d_out);
}
```

```cpp
#include <hip/hip_runtime.h>
#include <hip/hip_bf16.h>
#include <stdint.h>

typedef __bf16 bf16x8 __attribute__((ext_vector_type(8)));
typedef __bf16 bf16x2 __attribute__((ext_vector_type(2)));
typedef short s16x4 __attribute__((ext_vector_type(4)));
typedef float f32x2 __attribute__((ext_vector_type(2)));
typedef float f32x4 __attribute__((ext_vector_type(4)));
typedef float f32x16 __attribute__((ext_vector_type(16)));
typedef unsigned u32x2 __attribute__((ext_vector_type(2)));
typedef unsigned u32x4 __attribute__((ext_vector_type(4)));
#define LAS __attribute__((address_space(3)))
typedef LAS char* ldsp;

constexpr size_t WS_QF  = 0;
constexpr size_t WS_O   = WS_QF + 32ull * 64 * 1280;
constexpr size_t WS_KVP = WS_O + 32ull * 8 * 20480;
constexpr size_t WS_WOP = WS_KVP + 16ull * 32 * 840 * 4;
constexpr size_t WS_WPF = WS_WOP + 64ull * 25600;
constexpr size_t WS_G   = WS_WPF + 32ull * 64 * 32;
constexpr size_t WS_CNT = WS_G + 2048 * 4;

constexpr int ST_X = 144 * 128, ST_W = 3 * 64 * 160, ST = ST_X + ST_W;
constexpr int K1_LDS = 3 * ST;
constexpr int CSTR = 592;
constexpr int PHSTR = 328;
constexpr int L_PHIK = 144 * CSTR;
constexpr int L_GW = K1_LDS;
constexpr int L_WPH = L_GW + 2048;
constexpr int L_WO = L_WPH + 4096;
constexpr int K1_LDS_ALL = L_WO + 8192;
constexpr int L_FL = K1_LDS_ALL + 256;

#define STAMP(i)
#define STAMP_DECL
#define STAMP_OUT(kid)
#define STAMP_ARG
#define STAMP_PASS

#define SBAR() __builtin_amdgcn_sched_barrier(0)
#define VBAR(N) asm volatile("s_waitcnt vmcnt(%0)\n\ts_barrier" :: "i"(N) : "memory")
#define LBAR() asm volatile("s_waitcnt lgkmcnt(0)\n\ts_barrier" ::: "memory")
#define MFMA16(a, b, c) __builtin_amdgcn_mfma_f32_16x16x32_bf16(a, b, c, 0, 0, 0)
#define MFMA32(a, b, c) __builtin_amdgcn_mfma_f32_32x32x16_bf16(a, b, c, 0, 0, 0)

__device__ __forceinline__ void glds16(const char* sbase, unsigned voff, unsigned lds_dst) {
    unsigned sv;
    asm volatile("s_mov_b32 %0, m0\n\ts_mov_b32 m0, %3\n\ts_nop 0\n\tglobal_load_lds_dwordx4 %1, %2\n\ts_mov_b32 m0, %0"
                 : "=&s"(sv) : "v"(voff), "s"(sbase), "s"(lds_dst) : "memory");
}
__device__ __forceinline__ void glds16_sc1(const char* sbase, unsigned voff, unsigned lds_dst) {
    unsigned sv;
    asm volatile("s_mov_b32 %0, m0\n\ts_mov_b32 m0, %3\n\ts_nop 0\n\tglobal_load_lds_dwordx4 %1, %2 sc1\n\ts_mov_b32 m0, %0"
                 : "=&s"(sv) : "v"(voff), "s"(sbase), "s"(lds_dst) : "memory");
}
#define PUBH 6
#define WAE 0
#define ILVN 21
#define ILVR 1
#define SPL 6
#define TLBW 0
#define TLBW2 0
#define EPF 1
#define KST 0
#define CPF 1
#define BDELAY 0
#define STAG 1
#define WPF 1
#define OSC1 4
#define K1AUX 16
#define WSRSRC(ws) __builtin_amdgcn_make_buffer_rsrc((void*)(ws), 0, (int)(WS_CNT + 4096), 0x00020000)
#define ST128(rs, off, v) __builtin_amdgcn_raw_buffer_store_b128((v), rs, (unsigned)(off), 0, K1AUX)
#define ST64(rs, off, v) __builtin_amdgcn_raw_buffer_store_b64((v), rs, (unsigned)(off), 0, K1AUX)
#define ST32(rs, off, v) __builtin_amdgcn_raw_buffer_store_b32((v), rs, (unsigned)(off), 0, K1AUX)
__device__ __forceinline__ void touch(const char* sbase, unsigned voff, unsigned lds_dst) {
    unsigned sv;
    asm volatile("s_mov_b32 %0, m0\n\ts_mov_b32 m0, %3\n\ts_nop 0\n\tglobal_load_lds_dword %1, %2\n\ts_mov_b32 m0, %0"
                 : "=&s"(sv) : "v"(voff), "s"(sbase), "s"(lds_dst) : "memory");
}
template <int N, bool SC1>
__device__ __forceinline__ void gcopy(const char* sbase, unsigned voff, unsigned lds_dst) {
    unsigned sv;
#define GL_(o) "global_load_lds_dwordx4 %1, %2 offset:" #o
#define GC_(body) asm volatile("s_mov_b32 %0, m0\n\ts_mov_b32 m0, %3\n\ts_nop 0\n\t" body "s_mov_b32 m0, %0" : "=&s"(sv) : "v"(voff), "s"(sbase), "s"(lds_dst) : "memory")
    if constexpr (SC1) {
        if constexpr (N == 4) GC_(GL_(0) " sc1\n\t" GL_(1024) " sc1\n\t" GL_(2048) " sc1\n\t" GL_(3072) " sc1\n\t");
        else if constexpr (N == 2) GC_(GL_(0) " sc1\n\t" GL_(1024) " sc1\n\t");
        else GC_(GL_(0) " sc1\n\t");
    } else {
        if constexpr (N == 4) GC_(GL_(0) "\n\t" GL_(1024) "\n\t" GL_(2048) "\n\t" GL_(3072) "\n\t");
        else if constexpr (N == 2) GC_(GL_(0) "\n\t" GL_(1024) "\n\t");
        else GC_(GL_(0) "\n\t");
    }
#undef GL_
#undef GC_
}
__device__ __forceinline__ bf16x8 lds_b128(ldsp p) { return *(const LAS bf16x8*)p; }
__device__ __forceinline__ u32x2 lds_b64(ldsp p) { return *(const LAS u32x2*)p; }
__device__ __forceinline__ u32x2 lds_tr(ldsp p) {
    return __builtin_bit_cast(u32x2, __builtin_amdgcn_ds_read_tr16_b64_v4i16((LAS s16x4*)p));
}
__device__ __forceinline__ bf16x8 cat2(u32x2 a, u32x2 b) {
    u32x4 r = {a[0], a[1], b[0], b[1]};
    return __builtin_bit_cast(bf16x8, r);
}
__device__ __forceinline__ unsigned pk2(float lo, float hi) {
    bf16x2 r = {(__bf16)lo, (__bf16)hi};
    return __builtin_bit_cast(unsigned, r);
}
__device__ __forceinline__ float rbf(float x) { return (float)(__bf16)x; }
__device__ __forceinline__ float bflo(unsigned u) { return __uint_as_float(u << 16); }
__device__ __forceinline__ float bfhi(unsigned u) { return __uint_as_float(u & 0xffff0000u); }
__device__ __forceinline__ float dot2(unsigned a, unsigned b, float c) {
    return __builtin_amdgcn_fdot2_f32_bf16(__builtin_bit_cast(bf16x2, a), __builtin_bit_cast(bf16x2, b), c, false);
}
#define XHALF(v, A, B) do { auto r_ = __builtin_amdgcn_permlane32_swap(__float_as_uint(v), __float_as_uint(v), false, false); A = __uint_as_float(r_[0]); B = __uint_as_float(r_[1]); } while (0)
#define XQUAD(v, A, B) do { auto r_ = __builtin_amdgcn_permlane16_swap(__float_as_uint(v), __float_as_uint(v), false, false); A = __uint_as_float(r_[0]); B = __uint_as_float(r_[1]); } while (0)
__device__ __forceinline__ int krow_inv(int rho) {
    return (rho & 19) | ((rho & 4) << 1) | ((rho & 8) >> 1);
}
#define NF 0
__device__ __forceinline__ int kstage(int s, int koff) {
    if (s < NF) return s;
    int t = s - NF + koff; t = t >= 10 - NF ? t - (10 - NF) : t;
    return NF + t;
}
constexpr float LOG2E = 1.4426950408889634f;

__device__ __forceinline__ void feat_map(const f32x16& p, unsigned (&o)[10]) {
    float mx = fmaxf(fmaxf(p[0], p[1]), p[2]), mn = fminf(fminf(p[0], p[1]), p[2]);
    mx = fmaxf(fmaxf(mx, p[3]), p[4]); mn = fminf(fminf(mn, p[3]), p[4]);
    mx = fmaxf(fmaxf(mx, p[5]), p[6]); mn = fminf(fminf(mn, p[5]), p[6]);
    mx = fmaxf(fmaxf(mx, p[7]), p[8]); mn = fminf(fminf(mn, p[7]), p[8]);
    mx = fmaxf(mx, p[9]); mn = fminf(mn, p[9]);
    float a, b;
    XHALF(mx, a, b); mx = fmaxf(a, b);
    XHALF(mn, a, b); mn = fminf(a, b);
    const f32x2 cp = {LOG2E, LOG2E}, cn = {-LOG2E, -LOG2E};
    const f32x2 mxl = {-mx * LOG2E, -mx * LOG2E}, mnl = {mn * LOG2E, mn * LOG2E};
    f32x2 ep[5], en[5];
#pragma unroll
    for (int i = 0; i < 5; ++i) {
        const f32x2 v = {p[2 * i], p[2 * i + 1]};
        const f32x2 ap = __builtin_elementwise_fma(v, cp, mxl), an = __builtin_elementwise_fma(v, cn, mnl);
        ep[i] = f32x2{__builtin_amdgcn_exp2f(ap[0]), __builtin_amdgcn_exp2f(ap[1])};
        en[i] = f32x2{__builtin_amdgcn_exp2f(an[0]), __builtin_amdgcn_exp2f(an[1])};
    }
    const f32x2 sp2 = ((ep[0] + ep[1]) + (ep[2] + ep[3])) + ep[4], sn2 = ((en[0] + en[1]) + (en[2] + en[3])) + en[4];
    float sp = sp2[0] + sp2[1], sn = sn2[0] + sn2[1];
    XHALF(sp, a, b); sp = a + b;
    XHALF(sn, a, b); sn = a + b;
    const float rp = __builtin_amdgcn_rcpf(sp), rn = __builtin_amdgcn_rcpf(sn);
    const f32x2 rp2 = {rp, rp}, rn2 = {rn, rn};
#pragma unroll
    for (int i = 0; i < 5; ++i) {
        const f32x2 tp = ep[i] * rp2, tn = en[i] * rn2;
        o[i] = pk2(tp[0], tp[1]);
        o[5 + i] = pk2(tn[0], tn[1]);
    }
}

__device__ __forceinline__ void pg_set(ldsp p, unsigned v) { *(volatile LAS unsigned*)p = v; }
__device__ __forceinline__ u32x4 pg_ld(ldsp p) { return *(volatile LAS u32x4*)p; }
__device__ __forceinline__ unsigned pg_min(u32x4 v) { return (unsigned)__builtin_amdgcn_readfirstlane(min(min(v[0], v[1]), min(v[2], v[3]))); }
__device__ __forceinline__ void pg_wait(ldsp p, unsigned need) {
    while (pg_min(pg_ld(p)) < need) __builtin_amdgcn_s_sleep(1);
    asm volatile("" ::: "memory");
}
template <int CW> struct K1Map {
    static constexpr int WM = CW >> 1, SIDE = CW & 1;
    static constexpr int nloc(int i) { return (WM == 1 && i < 5) ? (i + 3) % 5 : i; }
    static constexpr int ntile(int i) { return SIDE ? 14 - nloc(i) : nloc(i); }
    static constexpr int mtile(int a) { return 4 * WM + (SIDE ? ((a + 2) & 3) : a); }
    static constexpr int woff(int i) { return ST_X + (ntile(i) / 5) * 10240 + (ntile(i) % 5) * 32; }
};

template <int CW, bool ISB>
__device__ __forceinline__ void k1_compute(ldsp lds, int lane, int koff, float* gout STAMP_ARG) {
    using M = K1Map<CW>;
    constexpr bool GATE = !ISB;
    constexpr int NH = ISB ? 3 - M::WM : 0;
    const int g = lane >> 4, l15 = lane & 15;
    const unsigned xs0 = (unsigned)(l15 * 128 + ((g ^ (lane & 7)) * 16));
    const unsigned wb = (unsigned)((16 * (g >> 1) + 4 * (g & 1) + ((lane >> 2) & 3)) * 160 + (lane & 3) * 8);
    f32x4 acc[8][4], hacc[4];
#pragma unroll
    for (int i = 0; i < 8; ++i)
#pragma unroll
        for (int a = 0; a < 4; ++a) acc[i][a] = f32x4{0.f, 0.f, 0.f, 0.f};
#pragma unroll
    for (int i = 0; i < 4; ++i) hacc[i] = f32x4{0.f, 0.f, 0.f, 0.f};
    unsigned gk = (unsigned)(L_GW + kstage(0, koff) * 128 + g * 16);
    bf16x8 xf0[4], wf0[8], xh0, xf1[4], wf1[8], xh1;
#define RD(XF, WF, XH, BUF, HALF) do { \
        ldsp px = lds + (BUF) + (xs0 ^ ((HALF) * 64)); ldsp pw = lds + (BUF) + wb; \
        _Pragma("unroll") for (int a = 0; a < 4; ++a) XF[a] = lds_b128(px + (16 + 16 * M::mtile(a)) * 128); \
        if (ISB) XH = lds_b128(px); \
        if (GATE) XH = lds_b128(lds + gk + (HALF) * 64); \
        _Pragma("unroll") for (int i = 0; i < 8; ++i) \
            WF[i] = cat2(lds_tr(pw + M::woff(i) + (HALF) * 5120), lds_tr(pw + M::woff(i) + (HALF) * 5120 + 1280)); \
    } while (0)
#define MM(XF, WF, XH) MMR(XF, WF, XH, 0, 8)
#define MMR(XF, WF, XH, I0, I1) do { \
        _Pragma("unroll") for (int i = I0; i < I1; ++i) { \
            _Pragma("unroll") for (int a = 0; a < 4; ++a) { \
                if (i < 7 || a < 2) acc[i][a] = MFMA16(WF[i], XF[a], acc[i][a]); } \
            if (ISB && i < NH) hacc[i] = MFMA16(WF[i], XH, hacc[i]); \
            if (GATE && i < 2) hacc[i] = MFMA16(XH, XF[2 + i], hacc[i]); \
        } \
    } while (0)
#define ILV() do { \
        _Pragma("unroll") for (int t_ = 0; t_ < ILVN; ++t_) { \
            __builtin_amdgcn_sched_group_barrier(0x008, 1, 0); __builtin_amdgcn_sched_group_barrier(0x100, ILVR, 0); } \
        __builtin_amdgcn_sched_group_barrier(0x008, 24, 0); \
    } while (0)
    LBAR();
    pg_wait(lds + L_FL, 1u);
    STAMP(2);
    unsigned cur = 0;
    RD(xf0, wf0, xh0, cur, 0);
    SBAR();
#pragma unroll 1
    for (int s = 0; s < 9; ++s) {
        RD(xf1, wf1, xh1, cur, 1);
        MMR(xf0, wf0, xh0, 0, SPL);
        ILV();
        SBAR();
        asm volatile("s_waitcnt lgkmcnt(0)" ::: "memory");
        pg_set(lds + L_FL + 16 + 4 * CW, (unsigned)(s + 1));
        const u32x4 fv = pg_ld(lds + L_FL);
        SBAR();
        MMR(xf0, wf0, xh0, SPL, 8);
        SBAR();
        cur = (cur == 2 * ST) ? 0u : cur + ST;
        if (GATE) gk = (unsigned)(L_GW + kstage(s + 1, koff) * 128 + g * 16);
        if (pg_min(fv) < (unsigned)(s + 2)) pg_wait(lds + L_FL, (unsigned)(s + 2));
        asm volatile("" ::: "memory");
        RD(xf0, wf0, xh0, cur, 0);
        MM(xf1, wf1, xh1);
        ILV();
        SBAR();
    }
    RD(xf1, wf1, xh1, cur, 1);
    MM(xf0, wf0, xh0);
    ILV();
    SBAR();
    MM(xf1, wf1, xh1);
#undef ILV
#undef RD
#undef MM
#undef MMR
    STAMP(3);
    LBAR();
    ldsp cb = lds + l15 * CSTR;
#pragma unroll
    for (int i = 0; i < 8; ++i) {
        const int cq = 4 * M::ntile(i) + g;
        ldsp cc = cb + 8 * (cq + (cq * 205 >> 10));
#pragma unroll
        for (int a = 0; a < 4; ++a) {
            if (i < 7 || a < 2) {
                u32x2 v = {pk2(acc[i][a][0], acc[i][a][1]), pk2(acc[i][a][2], acc[i][a][3])};
                *(LAS u32x2*)(cc + (16 + 16 * M::mtile(a)) * CSTR) = v;
            }
        }
        if (ISB && i < NH) {
            u32x2 v = {pk2(hacc[i][0], hacc[i][1]), pk2(hacc[i][2], hacc[i][3])};
            *(LAS u32x2*)(cc) = v;
        }
    }
    if (GATE && gout != nullptr) {
        const float z = rbf((g & 1) ? hacc[1][0] : hacc[0][0]);
        if (lane < 32) __hip_atomic_store(gout + 16 * M::mtile(2) + lane, rbf(__builtin_amdgcn_rcpf(1.f + __builtin_amdgcn_exp2f(-z * LOG2E))), __ATOMIC_RELAXED, __HIP_MEMORY_SCOPE_AGENT);
    }
}

template <int V> struct IC { static constexpr int value = V; };
template <int LW>
__device__ __forceinline__ void k1_loader(ldsp lds, unsigned ldsb, int lane, const char* x, const char* wsrc, bool isB, int r0, int hg, int koff,
                                          const char* gatew, const char* wphi, const char* woA, const char* woB, int b, const __amdgpu_buffer_rsrc_t wrs STAMP_ARG) {
    if (LW < 2) { const unsigned c = 64u * LW + lane; glds16(gatew, (c < 80u ? c : 79u) * 16u, ldsb + L_GW + LW * 1024); }
    const unsigned xo = (unsigned)((lane >> 3) * 1280 + (((lane & 7) ^ (lane >> 3)) * 16));
    const char* xb = x + (size_t)(r0 - 16) * 1280;
    const char* xh = x + (size_t)(r0 >= 16 ? r0 - 16 : 0) * 1280;
    unsigned wo[5];
    if (LW > 0) {
#pragma unroll
        for (int q = 0; q < 5; ++q) {
            const unsigned c = 64u * q + lane;
            const unsigned rho = (c * 205u) >> 11, ch = c - rho * 10u;
            wo[q] = (unsigned)krow_inv((int)rho) * 3840u + ch * 16u;
        }
    }
    const char* wbase = wsrc + hg * 160;
    const int sc0 = isB ? 1280 : 0, sc1 = isB ? 0 : 1280;
    auto issue = [&](int s, unsigned buf, auto I0, auto I1) {
        const int ks = kstage(s, koff);
#pragma unroll
        for (int i = I0.value; i < I1.value; ++i) {
            const int p = 12 * LW + i;
            if (p < 18) {
                const char* sb = (p < 2 ? xh : xb) + ks * 128 + p * 10240;
                if (p >= 2 || isB) glds16(sb, xo, ldsb + buf + p * 1024);
            } else {
                const int q = p - 18, seg = q / 10, hf = (q % 10) / 5;
                const char* sb = wbase + (size_t)ks * (64 * 3840) + hf * (32 * 3840) + (seg == 0 ? sc0 : (seg == 1 ? sc1 : 2560));
                glds16(sb, wo[q % 5], ldsb + buf + p * 1024);
            }
        }
    };
    unsigned cur = 0;
#pragma unroll 1
    for (int s = 0; s < 10; ++s) {
        if (s >= 3) pg_wait(lds + L_FL + 16, (unsigned)(s - 2));
        issue(s, cur, IC<0>{}, IC<PUBH>{});
        if (s > 0) {
            if (LW == 0 && !isB) asm volatile("s_waitcnt vmcnt(%0)" :: "i"(PUBH - 2) : "memory"); else asm volatile("s_waitcnt vmcnt(%0)" :: "i"(PUBH) : "memory");
            pg_set(lds + L_FL + 4 * LW, (unsigned)s);
        }
        issue(s, cur, IC<PUBH>{}, IC<12>{});
        cur = (cur == 2 * ST) ? 0u : cur + ST;
        if (s == 0) asm volatile("s_barrier" ::: "memory");
        if (s == 3) {
            const unsigned c = 64u * LW + lane;
            glds16(wphi + hg * 3200, (c < 200u ? c : 199u) * 16u, ldsb + L_WPH + LW * 1024);
            const int bb = b < 200 ? b : 199;
            const int kb = bb / 5, ng = bb - kb * 5, st = kb / 5, j = kb - st * 5;
            const char* wo = ((st >> 2) ? woA : woB) + (size_t)(8 * (st & 3)) * 20 * 1280 + ng * 256;
#pragma unroll
            for (int i = 0; i < 2; ++i) {
                const int kk = 8 * LW + 4 * i + (lane >> 4), e = kk & 7;
                const int head = j < 4 ? 2 * j + (LW >> 1) : 2 * LW + (e >> 2);
                const int d = j < 4 ? 8 * (LW & 1) + e : 16 + (e & 3);
                glds16(wo, (unsigned)((head * 20 + d) * 1280 + (lane & 15) * 16), ldsb + L_WO + (2 * LW + i) * 1024);
            }
        }
    }
}

__global__ __launch_bounds__(512, 2) void k1_qkv(const __hip_bfloat16* __restrict__ x, const __hip_bfloat16* __restrict__ wA,
                                                   const __hip_bfloat16* __restrict__ wB, const __hip_bfloat16* __restrict__ wphi,
                                                   const __hip_bfloat16* __restrict__ gatew, const __hip_bfloat16* __restrict__ woA,
                                                   const __hip_bfloat16* __restrict__ woB, char* __restrict__ ws, const __hip_bfloat16* outp) {
    __shared__ __attribute__((aligned(16))) char smem[K1_LDS_ALL + 256 + 128];
    ldsp lds = (ldsp)smem;
    STAMP_DECL STAMP(0);
    const int tid = threadIdx.x, lane = tid & 63;
    if (tid < 64) {
        unsigned long long pc0; unsigned sv;
        asm volatile("s_getpc_b64 %1\n\ts_mov_b32 %0, m0\n\ts_mov_b32 m0, %3\n\ts_nop 0\n\tglobal_load_lds_dword %2, %1\n\ts_mov_b32 m0, %0"
                     : "=&s"(sv), "=&s"(pc0) : "v"(tid * 128), "s"((unsigned)(uintptr_t)lds + K1_LDS_ALL) : "memory");
    }
    asm volatile("" :: "s"(x), "s"(wA), "s"(wB), "s"(wphi), "s"(gatew), "s"(woA), "s"(woB), "s"(ws));
    const int w = __builtin_amdgcn_readfirstlane(tid >> 6);
    const int b = blockIdx.x, xcd = b & 7, idx = b >> 3;
    const int rt = 8 * (xcd & 1) + (idx & 7);
    const int cg = 4 * (xcd >> 1) + (idx >> 3);
    const bool isB = cg >= 8;
    const int hg = cg & 7;
    const int r0 = rt * 128;
    const int r32 = lane & 31, hh = lane >> 5;
    const __amdgpu_buffer_rsrc_t wrs = WSRSRC(ws);
    const int koff = (3 * (idx >> 3) + (idx & 7)) % (10 - NF);

    if (w < 4) {
        {
            unsigned long long pc; int len;
            asm volatile("s_getpc_b64 %0\n.Lgm_here:\n\ts_mov_b32 %1, .Lgm_k2end - .Lgm_here" : "=s"(pc), "=s"(len));
            const int off = idx * 2048 + w * 512 + (lane & 3) * 128;
            if (lane < 4 && off < len) {
                unsigned sv;
                asm volatile("s_mov_b32 %0, m0\n\ts_mov_b32 m0, %3\n\ts_nop 0\n\tglobal_load_lds_dword %1, %2\n\ts_mov_b32 m0, %0"
                             : "=&s"(sv) : "v"(off), "s"(pc), "s"((unsigned)(uintptr_t)lds + K1_LDS_ALL) : "memory");
            }
        }
        if (tid < 32) *(volatile LAS unsigned*)(lds + L_FL + tid * 4) = 0u;
        float* gout = cg == 0 ? (float*)(ws + WS_G) + r0 : nullptr;
        STAMP(1);
        if (isB) {
            if (w == 0) k1_compute<0, true>(lds, lane, koff, gout STAMP_PASS); else if (w == 1) k1_compute<1, true>(lds, lane, koff, gout STAMP_PASS);
            else if (w == 2) k1_compute<2, true>(lds, lane, koff, gout STAMP_PASS); else k1_compute<3, true>(lds, lane, koff, gout STAMP_PASS);
        } else {
            if (w == 0) k1_compute<0, false>(lds, lane, koff, gout STAMP_PASS); else if (w == 1) k1_compute<1, false>(lds, lane, koff, gout STAMP_PASS);
            else if (w == 2) k1_compute<2, false>(lds, lane, koff, gout STAMP_PASS); else k1_compute<3, false>(lds, lane, koff, gout STAMP_PASS);
        }
    } else {
        const unsigned ldsb = (unsigned)(uintptr_t)lds;
        const char* wsrc = (const char*)(isB ? wB : wA);
        if (w == 4) k1_loader<0>(lds, ldsb, lane, (const char*)x, wsrc, isB, r0, hg, koff, (const char*)gatew, (const char*)wphi, (const char*)woA, (const char*)woB, b, wrs STAMP_PASS);
        else if (w == 5) k1_loader<1>(lds, ldsb, lane, (const char*)x, wsrc, isB, r0, hg, koff, (const char*)gatew, (const char*)wphi, (const char*)woA, (const char*)woB, b, wrs STAMP_PASS);
        else if (w == 6) k1_loader<2>(lds, ldsb, lane, (const char*)x, wsrc, isB, r0, hg, koff, (const char*)gatew, (const char*)wphi, (const char*)woA, (const char*)woB, b, wrs STAMP_PASS);
        else k1_loader<3>(lds, ldsb, lane, (const char*)x, wsrc, isB, r0, hg, koff, (const char*)gatew, (const char*)wphi, (const char*)woA, (const char*)woB, b, wrs STAMP_PASS);
        asm volatile("s_waitcnt vmcnt(0)" ::: "memory");
        pg_set(lds + L_FL + 4 * (w - 4), 10u);
        pg_wait(lds + L_FL, 6u);
        const int lw = w - 4;
        u32x4 wof[2];
#pragma unroll
        for (int i = 0; i < 2; ++i) {
            ldsp pw = lds + L_WO + (8 * (lane >> 4) + ((lane >> 2) & 3)) * 256 + (2 * lw + i) * 32 + (lane & 3) * 8;
            const u32x2 f0 = lds_tr(pw), f1 = lds_tr(pw + 1024);
            wof[i] = u32x4{f0[0], f0[1], f1[0], f1[1]};
        }
        u32x4 t0, t1;
        {
            const int rho = (r32 & 3) + 4 * (r32 >> 3), ho = (r32 >> 2) & 1;
            const bool vr = rho < 10, v1 = vr && hh == 0;
            const LAS unsigned short* wpp = (const LAS unsigned short*)(lds + L_WPH + lw * 800) + (vr ? 10 * ho + rho : 0);
#pragma unroll
            for (int j = 0; j < 4; ++j) { const unsigned v = (unsigned)wpp[(8 * hh + 2 * j) * 20] | ((unsigned)wpp[(8 * hh + 2 * j + 1) * 20] << 16); t0[j] = vr ? v : 0u; }
#pragma unroll
            for (int j = 0; j < 2; ++j) { const unsigned v = (unsigned)wpp[(16 + 2 * j) * 20] | ((unsigned)wpp[(17 + 2 * j) * 20] << 16); t1[j] = v1 ? v : 0u; }
            t1[2] = 0u; t1[3] = 0u;
        }
        STAMP(3);
        {
            const u32x4 sent = {0xffffffffu, 0xffffffffu, 0xffffffffu, 0xffffffffu};
            const unsigned dm = (unsigned)WS_O + ((b >> 3) * 8 + 4 + ((b >> 1) & 3)) * 20480 + (b & 1) * 10240 + lw * 1024 + lane * 16;
            ST128(wrs, dm, sent); ST128(wrs, dm + 4096, sent);
            if (lw < 2) ST128(wrs, dm + 8192, sent);
        }
        if (b < 200) {
            const int kb = b / 5, ng = b - kb * 5, st = kb / 5, j = kb - st * 5;
#pragma unroll
            for (int i = 0; i < 2; ++i) {
                const int nt = 8 * ng + 2 * lw + i, ct = nt / 5, n = nt - ct * 5;
                ST128(wrs, (unsigned)WS_WOP + (((ct * 8 + st) * 5 + j) * 5 + n) * 1024 + lane * 16, wof[i]);
            }
        }
        if (!isB && rt == 0) {
            const unsigned dst = (unsigned)WS_WPF + (4 * hg + lw) * 2048 + lane * 16;
            ST128(wrs, dst, t0);
            ST128(wrs, dst + 1024, t1);
        }
        LBAR();
        *(LAS u32x4*)(lds + L_WO + lw * 1024 + lane * 16) = t0;
        *(LAS u32x2*)(lds + L_WO + 4096 + lw * 512 + lane * 8) = u32x2{t1[0], t1[1]};
    }
    LBAR();
    STAMP(4);
    if (!isB) {
        const int hl = w & 3, rp = w >> 2;
        const int h = 4 * hg + hl;
        const u32x2 z2 = {0u, 0u};
        const bf16x8 wa0 = lds_b128(lds + L_WO + hl * 1024 + lane * 16);
        const bf16x8 wa1 = cat2(lds_b64(lds + L_WO + 4096 + hl * 512 + lane * 8), z2);
        f32x16 p[2];
#pragma unroll
        for (int u = 0; u < 2; ++u) {
            ldsp cp = lds + (16 + 32 * (2 * rp + u) + r32) * CSTR + 192 + 48 * hl;
            bf16x8 b0 = lds_b128(cp + hh * 16);
            u32x2 t = lds_b64(cp + 32);
            if (hh) t = z2;
            p[u] = MFMA32(wa0, b0, (f32x16{}));
            p[u] = MFMA32(wa1, cat2(t, z2), p[u]);
        }
#pragma unroll
        for (int u = 0; u < 2; ++u) {
            ldsp cp = lds + (16 + 32 * (2 * rp + u) + r32) * CSTR + 48 * hl;
            const u32x4 a0 = *(const LAS u32x4*)(cp + hh * 16); const u32x2 t = lds_b64(cp + 32);
            const unsigned dst = (unsigned)WS_QF + (h * 64 + (rt * 4 + 2 * rp + u)) * 1280;
            ST128(wrs, dst + lane * 16, a0);
            if (hh == 0) ST64(wrs, dst + 1024 + r32 * 8, t);
        }
#pragma unroll
        for (int u = 0; u < 2; ++u) {
            unsigned o[10];
            feat_map(p[u], o);
            ldsp pp = lds + L_PHIK + (32 * (2 * rp + u) + r32) * PHSTR + hl * 80 + hh * 40;
#pragma unroll
            for (int i = 0; i < 5; ++i) *(LAS u32x2*)(pp + 8 * i) = u32x2{o[2 * i], o[2 * i + 1]};
        }
        LBAR();
        STAMP(5);
        {
            const int nt = w >> 2;
            const int g = lane >> 4, q = (lane >> 2) & 3, pp = lane & 3, l15 = lane & 15;
            const int rowl = 4 * (4 * (g & 1) + q) + 2 * (g >> 1);
            ldsp pa = lds + L_PHIK + rowl * PHSTR + hl * 80 + pp * 8;
            ldsp pv = lds + (16 + rowl) * CSTR + 384 + 48 * hl + 32 * nt + 8 * pp;
            f32x4 kv[3];
#pragma unroll
            for (int m = 0; m < 3; ++m) kv[m] = f32x4{0.f, 0.f, 0.f, 0.f};
            const bool ones = (nt == 1) && (l15 == 4);
#pragma unroll
            for (int kk = 0; kk < 4; ++kk) {
                u32x2 v0 = lds_tr(pv + kk * 32 * CSTR), v1 = lds_tr(pv + kk * 32 * CSTR + CSTR);
                if (ones) { v0 = u32x2{0x3f803f80u, 0x3f803f80u}; v1 = v0; }
                bf16x8 vb = cat2(v0, v1);
#pragma unroll
                for (int m = 0; m < 3; ++m) {
                    bf16x8 af = cat2(lds_tr(pa + kk * 32 * PHSTR + m * 32), lds_tr(pa + kk * 32 * PHSTR + PHSTR + m * 32));
                    kv[m] = MFMA16(af, vb, kv[m]);
                }
            }
            const int d = 16 * nt + l15;
            const unsigned dst = (unsigned)WS_KVP + ((rt * 32 + h) * 840 + d * 40 + 4 * g) * 2;
            if (d < 21) {
                ST64(wrs, dst, (u32x2{pk2(kv[0][0], kv[0][1]), pk2(kv[0][2], kv[0][3])}));
                ST64(wrs, dst + 32, (u32x2{pk2(kv[1][0], kv[1][1]), pk2(kv[1][2], kv[1][3])}));
                if (g < 2) ST64(wrs, dst + 64, (u32x2{pk2(kv[2][0], kv[2][1]), pk2(kv[2][2], kv[2][3])}));
            }
        }
    } else {
        const int hl = w & 3, par = w >> 2;
        const int g = lane >> 4, l15 = lane & 15;
        const u32x2 z2 = {0u, 0u};
        const int gc = g < 2 ? g : 2;
        ldsp kq = lds + (16 + 2 * l15 + par) * CSTR + 48 * hl + 16 * gc;
        bf16x8 kf[5], qf[4];
        auto fixk = [&](u32x4 v) { if (g >= 2) { v[2] = 0u; v[3] = 0u; } if (g == 3) { v[0] = 0u; v[1] = 0u; } return __builtin_bit_cast(bf16x8, v); };
        kf[0] = fixk(*(const LAS u32x4*)(lds + (l15 >= 8 ? 2 * l15 - 16 + par : par) * CSTR + 48 * hl + 16 * gc));
#pragma unroll
        for (int kb = 0; kb < 4; ++kb) {
            kf[kb + 1] = fixk(*(const LAS u32x4*)(kq + kb * 32 * CSTR));
            qf[kb] = fixk(*(const LAS u32x4*)(kq + 192 + kb * 32 * CSTR));
        }
        u32x2 vt[5][2];
        {
            const int q4 = (lane >> 2) & 3, pp = lane & 3, key = 4 * g + q4;
            ldsp vp = lds + (16 + 2 * key + par) * CSTR + 384 + 48 * hl + 8 * pp;
            ldsp v0 = lds + (key >= 8 ? 2 * key - 16 + par : par) * CSTR + 384 + 48 * hl + 8 * pp;
            vt[0][0] = lds_tr(v0); vt[0][1] = lds_tr(v0 + 32);
#pragma unroll
            for (int kb = 0; kb < 4; ++kb) { vt[kb + 1][0] = lds_tr(vp + kb * 32 * CSTR); vt[kb + 1][1] = lds_tr(vp + kb * 32 * CSTR + 32); }
        }
        const float NEG = -__builtin_inff();
        float b0[4], b1[4];
#pragma unroll
        for (int e = 0; e < 4; ++e) {
            const int t = 4 * g + e;
            b0[e] = (t >= l15 + 9) ? 0.f : NEG;
            b1[e] = (t <= l15 && t >= l15 - 7) ? 0.f : NEG;
        }
        const float sc = 0.22360679774997896f * LOG2E;
        const int hB = 4 * hg + hl;
        const f32x4 zero4 = {0.f, 0.f, 0.f, 0.f};
        f32x4 s0[4], s1[4];
#pragma unroll
        for (int qb = 0; qb < 4; ++qb) { s0[qb] = MFMA16(kf[qb], qf[qb], zero4); s1[qb] = MFMA16(kf[qb + 1], qf[qb], zero4); }
        float mx[4], sum[4];
#pragma unroll
        for (int qb = 0; qb < 4; ++qb) {
#pragma unroll
            for (int e = 0; e < 4; ++e) {
                s0[qb][e] = fmaf(s0[qb][e], sc, (qb == 0 && rt == 0) ? NEG : b0[e]);
                s1[qb][e] = fmaf(s1[qb][e], sc, b1[e]);
            }
            mx[qb] = fmaxf(fmaxf(fmaxf(s0[qb][0], s0[qb][1]), fmaxf(s0[qb][2], s0[qb][3])), fmaxf(fmaxf(s1[qb][0], s1[qb][1]), fmaxf(s1[qb][2], s1[qb][3])));
        }
#pragma unroll
        for (int qb = 0; qb < 4; ++qb) { float a, bb; XQUAD(mx[qb], a, bb); mx[qb] = fmaxf(a, bb); }
#pragma unroll
        for (int qb = 0; qb < 4; ++qb) { float a, bb; XHALF(mx[qb], a, bb); mx[qb] = fmaxf(a, bb); }
#pragma unroll
        for (int qb = 0; qb < 4; ++qb) {
#pragma unroll
            for (int e = 0; e < 4; ++e) { s0[qb][e] = __builtin_amdgcn_exp2f(s0[qb][e] - mx[qb]); s1[qb][e] = __builtin_amdgcn_exp2f(s1[qb][e] - mx[qb]); }
            const f32x4 t = s0[qb] + s1[qb];
            sum[qb] = (t[0] + t[1]) + (t[2] + t[3]);
        }
#pragma unroll
        for (int qb = 0; qb < 4; ++qb) { float a, bb; XQUAD(sum[qb], a, bb); sum[qb] = a + bb; }
#pragma unroll
        for (int qb = 0; qb < 4; ++qb) { float a, bb; XHALF(sum[qb], a, bb); sum[qb] = __builtin_amdgcn_rcpf(a + bb); }
        u32x2 om[4], ot[4];
#pragma unroll
        for (int qb = 0; qb < 4; ++qb) {
            const f32x4 p0 = s0[qb] * sum[qb], p1 = s1[qb] * sum[qb];
            const bf16x8 pf = cat2(u32x2{pk2(p0[0], p0[1]), pk2(p0[2], p0[3])}, u32x2{pk2(p1[0], p1[1]), pk2(p1[2], p1[3])});
            const f32x4 o0 = MFMA16(cat2(vt[qb][0], vt[qb + 1][0]), pf, zero4);
            const f32x4 o1 = MFMA16(cat2(vt[qb][1], vt[qb + 1][1]), pf, zero4);
            om[qb] = u32x2{pk2(o0[0], o0[1]), pk2(o0[2], o0[3])};
            ot[qb] = u32x2{pk2(o1[0], o1[1]), pk2(o1[2], o1[3])};
        }
        const unsigned ob = (unsigned)WS_O + (2 * rt * 8 + (hB >> 3)) * 20480;
        const unsigned slot = (unsigned)(par * 32 + l15);
#pragma unroll
        for (int qb = 0; qb < 4; ++qb) ST64(wrs, ob + (qb >> 1) * (8 * 20480) + (hB & 7) * 2048 + (slot + 16 * (qb & 1)) * 32 + g * 8, om[qb]);
        if (g == 0) {
#pragma unroll
            for (int qb = 0; qb < 4; ++qb) ST64(wrs, ob + (qb >> 1) * (8 * 20480) + 16384 + (hB & 7) * 512 + (slot + 16 * (qb & 1)) * 8, ot[qb]);
        }
        STAMP(5);
    }
    STAMP_OUT(0);
}

constexpr int K2_LDS = 8 * 20480;
constexpr int K2_KV = 4 * 20480;
constexpr int K2_FL = K2_KV + 1792;
__device__ __forceinline__ u32x4 bld128(__amdgpu_buffer_rsrc_t rs, unsigned voff, unsigned soff, bool sc1) {
    return sc1 ? __builtin_amdgcn_raw_buffer_load_b128(rs, voff, soff, 16) : __builtin_amdgcn_raw_buffer_load_b128(rs, voff, soff, 0);
}
template <bool SC1>
__device__ __forceinline__ void k2_ldx(__amdgpu_buffer_rsrc_t ro, unsigned xm, unsigned xt, int j, u32x4 (&xf)[4]) {
#pragma unroll
    for (int m = 0; m < 4; ++m) {
        if (j < 4) xf[m] = bld128(ro, xm + m * 512, j * 4096, SC1);
        else {
            const u32x2 t0 = SC1 ? __builtin_amdgcn_raw_buffer_load_b64(ro, xt + m * 128, 16384, 16) : __builtin_amdgcn_raw_buffer_load_b64(ro, xt + m * 128, 16384, 0);
            const u32x2 t1 = SC1 ? __builtin_amdgcn_raw_buffer_load_b64(ro, xt + m * 128 + 512, 16384, 16) : __builtin_amdgcn_raw_buffer_load_b64(ro, xt + m * 128 + 512, 16384, 0);
            xf[m] = u32x4{t0[0], t0[1], t1[0], t1[1]};
        }
    }
}
__device__ __forceinline__ void k2_ldw(__amdgpu_buffer_rsrc_t rw, unsigned lo, int j, u32x4 (&wf)[5]) {
#pragma unroll
    for (int n = 0; n < 5; ++n) wf[n] = bld128(rw, lo + (n & 3) * 1024, j * 5120 + (n >> 2) * 4096, false);
}
__device__ __forceinline__ void k2_mm(const u32x4 (&xf)[4], const u32x4 (&wf)[5], f32x4 (&acc)[4][5]) {
#pragma unroll
    for (int m = 0; m < 4; ++m)
#pragma unroll
        for (int n = 0; n < 5; ++n) acc[m][n] = MFMA16(__builtin_bit_cast(bf16x8, wf[n]), __builtin_bit_cast(bf16x8, xf[m]), acc[m][n]);
}
__device__ __forceinline__ unsigned k2_max(const u32x4 (&xf)[4], unsigned mx) {
#pragma unroll
    for (int m = 0; m < 4; ++m) { mx = max(max(mx, xf[m][0]), xf[m][1]); mx = max(max(mx, xf[m][2]), xf[m][3]); }
    return mx;
}

__global__ __launch_bounds__(512, 2) void k2_out(char* __restrict__ ws, __hip_bfloat16* __restrict__ out) {
    __shared__ __attribute__((aligned(16))) char smem[K2_LDS];
    ldsp lds = (ldsp)smem;
    STAMP_DECL STAMP(0);
    const int tid = threadIdx.x, lane = tid & 63;
    const int w = __builtin_amdgcn_readfirstlane(tid >> 6);
    const int b = blockIdx.x, xcd = b & 7, idx = b >> 3;
    const int rt = 8 * (xcd & 3) + (idx & 7);
    const int ct = 4 * (xcd >> 2) + (idx >> 3);
    const int r0 = rt * 64;
    const int g = lane >> 4, l15 = lane & 15, r32 = lane & 31, hh = lane >> 5;
    const u32x2 z2 = {0u, 0u};
    const __amdgpu_buffer_rsrc_t ro = __builtin_amdgcn_make_buffer_rsrc(ws + WS_O + (size_t)(rt * 8 + w) * 20480, 0, 20480, 0x00020000);
    const __amdgpu_buffer_rsrc_t rw = __builtin_amdgcn_make_buffer_rsrc(ws + WS_WOP + (size_t)(ct * 8 + w) * 25600, 0, 25600, 0x00020000);
    const unsigned xm = (unsigned)((g >> 1) * 2048 + l15 * 32 + (g & 1) * 16);
    const unsigned xt = (unsigned)(2 * g * 512 + l15 * 8);
    const unsigned lo = (unsigned)lane * 16u;
    f32x4 acc[4][5];
#pragma unroll
    for (int m = 0; m < 4; ++m)
#pragma unroll
        for (int n = 0; n < 5; ++n) acc[m][n] = f32x4{0.f, 0.f, 0.f, 0.f};
    float gv[4];

    if (w >= 4) {
        const int pw = w - 4, pt = tid - 256;
        const int h = b & 31, c = b >> 5;
        u32x4 wf[3][5], xf[3][4];
        {
            u32x4 q0[2]; u32x2 q1[2];
#pragma unroll
            for (int u = 0; u < 2; ++u) {
                const char* pq = ws + WS_QF + ((size_t)h * 64 + c * 8 + 2 * pw + u) * 1280;
                q0[u] = *(const u32x4*)(pq + lane * 16);
                q1[u] = *(const u32x2*)(pq + 1024 + r32 * 8);
            }
            const u32x4 wa0 = *(const u32x4*)(ws + WS_WPF + (size_t)h * 2048 + lane * 16);
            const u32x4 wa1 = *(const u32x4*)(ws + WS_WPF + (size_t)h * 2048 + 1024 + lane * 16);
            u32x2 t[16];
            {
                const int tc = pt < 210 ? pt : 209;
                const char* kp = ws + WS_KVP + (size_t)h * 1680 + tc * 8;
#pragma unroll
                for (int r = 0; r < 16; ++r) t[r] = *(const u32x2*)(kp + (size_t)r * (32 * 1680));
            }
            if (pt < 2) *(volatile LAS unsigned*)(lds + K2_FL + 4 * pt) = 0u;
            LBAR();
            unsigned ph[2][10];
#pragma unroll
            for (int u = 0; u < 2; ++u) {
                u32x2 tq = q1[u]; if (hh) tq = z2;
                f32x16 p = MFMA32(__builtin_bit_cast(bf16x8, wa0), __builtin_bit_cast(bf16x8, q0[u]), (f32x16{}));
                p = MFMA32(__builtin_bit_cast(bf16x8, wa1), cat2(tq, z2), p);
                feat_map(p, ph[u]);
            }
            SBAR();
            f32x4 ks = {0.f, 0.f, 0.f, 0.f};
#pragma unroll
            for (int r = 0; r < 16; ++r) ks += f32x4{bflo(t[r][0]), bfhi(t[r][0]), bflo(t[r][1]), bfhi(t[r][1])};
            if (pt < 210) *(LAS u32x2*)(lds + K2_KV + pt * 8) = u32x2{pk2(ks[0], ks[1]), pk2(ks[2], ks[3])};
            asm volatile("s_waitcnt lgkmcnt(0)" ::: "memory");
            if (lane == 0) __hip_atomic_fetch_add((LAS unsigned*)(lds + K2_FL), 1u, __ATOMIC_RELAXED, __HIP_MEMORY_SCOPE_WORKGROUP);
            STAMP(1);
            while ((unsigned)__builtin_amdgcn_readfirstlane(__hip_atomic_load((LAS unsigned*)(lds + K2_FL), __ATOMIC_RELAXED, __HIP_MEMORY_SCOPE_WORKGROUP)) < 4u) __builtin_amdgcn_s_sleep(1);
            asm volatile("" ::: "memory");
            const int rho = (r32 & 3) + 4 * (r32 >> 3), ho = (r32 >> 2) & 1;
            const bool vr = rho <= 10;
            const int d = rho < 8 ? 8 * ho + rho : (rho < 10 ? 16 + 2 * ho + (rho - 8) : 20);
            ldsp ap = lds + K2_KV + d * 80 + hh * 40;
            u32x2 a00 = lds_b64(ap), a01 = lds_b64(ap + 8), a10 = lds_b64(ap + 16), a11 = lds_b64(ap + 24), a20 = lds_b64(ap + 32);
            asm volatile("s_waitcnt lgkmcnt(0)" ::: "memory");
            if (lane == 0) __hip_atomic_fetch_add((LAS unsigned*)(lds + K2_FL + 4), 1u, __ATOMIC_RELAXED, __HIP_MEMORY_SCOPE_WORKGROUP);
            if (!vr) { a00 = z2; a01 = z2; a10 = z2; a11 = z2; a20 = z2; }
            const __amdgpu_buffer_rsrc_t rs = __builtin_amdgcn_make_buffer_rsrc(ws + WS_O + (size_t)((c * 4 + pw) * 8 + 4 + (h >> 3)) * 20480, 0, 20480, 0x00020000);
#pragma unroll
            for (int u = 0; u < 2; ++u) {
                f32x16 p = MFMA32(cat2(a00, a01), cat2(u32x2{ph[u][0], ph[u][1]}, u32x2{ph[u][2], ph[u][3]}), (f32x16{}));
                p = MFMA32(cat2(a10, a11), cat2(u32x2{ph[u][4], ph[u][5]}, u32x2{ph[u][6], ph[u][7]}), p);
                p = MFMA32(cat2(a20, z2), cat2(u32x2{ph[u][8], ph[u][9]}, z2), p);
                const float rd = __builtin_amdgcn_rcpf(p[10] + 1e-6f);
                const int srow = (r32 & 1) * 32 + u * 16 + (r32 >> 1);
                u32x4 m = {pk2(p[0] * rd, p[1] * rd), pk2(p[2] * rd, p[3] * rd), pk2(p[4] * rd, p[5] * rd), pk2(p[6] * rd, p[7] * rd)};
                __builtin_amdgcn_raw_buffer_store_b128(m, rs, (unsigned)((h & 7) * 2048 + srow * 32 + hh * 16), 0, 16);
                __builtin_amdgcn_raw_buffer_store_b32(pk2(p[8] * rd, p[9] * rd), rs, (unsigned)(16384 + (h & 7) * 512 + srow * 8 + hh * 4), 0, 16);
            }
            STAMP(2);
        }
        SBAR();
#pragma unroll
        for (int j = 0; j < 3; ++j) k2_ldw(rw, lo, j, wf[j]);
#pragma unroll
        for (int m = 0; m < 4; ++m) gv[m] = ((const float*)(ws + WS_G))[r0 + 32 * (m & 1) + 2 * l15 + (m >> 1)];
        SBAR();
        int redo = 0;
#pragma unroll 1
        for (;;) {
            asm volatile("" ::: "memory");
#pragma unroll
            for (int j = 0; j < 3; ++j) k2_ldx<true>(ro, xm, xt, j, xf[j]);
            unsigned mx = 0u;
#pragma unroll
            for (int j = 0; j < 3; ++j) mx = k2_max(xf[j], mx);
            if (__builtin_amdgcn_ballot_w64(mx == 0xffffffffu) == 0ull || ++redo > (1 << 16)) break;
            __builtin_amdgcn_s_sleep(2);
        }
        STAMP(3);
#pragma unroll
        for (int j = 0; j < 3; ++j) k2_mm(xf[j], wf[j], acc);
        if (w == 4) {
            while ((unsigned)__builtin_amdgcn_readfirstlane(__hip_atomic_load((LAS unsigned*)(lds + K2_FL + 4), __ATOMIC_RELAXED, __HIP_MEMORY_SCOPE_WORKGROUP)) < 4u) __builtin_amdgcn_s_sleep(1);
            asm volatile("" ::: "memory");
        }
        STAMP(4);
#pragma unroll
        for (int m = 0; m < 4; ++m)
#pragma unroll
            for (int n = 0; n < 5; ++n) *(LAS f32x4*)(lds + w * 20480 + (m * 5 + n) * 1024 + lo) = acc[m][n] * gv[m];
    } else {
        asm volatile("s_barrier" ::: "memory");
        {
            u32x4 wf[5][5], xf[5][4];
#pragma unroll
            for (int j = 0; j < 3; ++j) { k2_ldx<false>(ro, xm, xt, j, xf[j]); k2_ldw(rw, lo, j, wf[j]); }
#pragma unroll
            for (int m = 0; m < 4; ++m) gv[m] = ((const float*)(ws + WS_G))[r0 + 32 * (m & 1) + 2 * l15 + (m >> 1)];
            SBAR();
            k2_mm(xf[0], wf[0], acc);
            STAMP(1);
            SBAR();
            k2_ldx<false>(ro, xm, xt, 3, xf[3]); k2_ldw(rw, lo, 3, wf[3]);
            SBAR();
            k2_mm(xf[1], wf[1], acc);
            SBAR();
            k2_ldx<false>(ro, xm, xt, 4, xf[4]); k2_ldw(rw, lo, 4, wf[4]);
            SBAR();
            k2_mm(xf[2], wf[2], acc);
            k2_mm(xf[3], wf[3], acc);
            k2_mm(xf[4], wf[4], acc);
            STAMP(2);
        }
        SBAR();
        const __amdgpu_buffer_rsrc_t ro2 = __builtin_amdgcn_make_buffer_rsrc(ws + WS_O + (size_t)(rt * 8 + w + 4) * 20480, 0, 20480, 0x00020000);
        const __amdgpu_buffer_rsrc_t rw2 = __builtin_amdgcn_make_buffer_rsrc(ws + WS_WOP + (size_t)(ct * 8 + w + 4) * 25600, 0, 25600, 0x00020000);
        u32x4 wf[2][5], xf[2][4];
        k2_ldw(rw2, lo, 3, wf[0]); k2_ldw(rw2, lo, 4, wf[1]);
        f32x4 accb[4][5];
#pragma unroll
        for (int m = 0; m < 4; ++m) {
            const float gb = rbf(1.f - gv[m]);
#pragma unroll
            for (int n = 0; n < 5; ++n) { accb[m][n] = acc[m][n] * gb; acc[m][n] = f32x4{0.f, 0.f, 0.f, 0.f}; }
        }
        SBAR();
        int redo = 0;
#pragma unroll 1
        for (;;) {
            asm volatile("" ::: "memory");
            k2_ldx<true>(ro2, xm, xt, 3, xf[0]); k2_ldx<true>(ro2, xm, xt, 4, xf[1]);
            const unsigned mx = k2_max(xf[1], k2_max(xf[0], 0u));
            if (__builtin_amdgcn_ballot_w64(mx == 0xffffffffu) == 0ull || ++redo > (1 << 16)) break;
            __builtin_amdgcn_s_sleep(2);
        }
        STAMP(3);
        k2_mm(xf[0], wf[0], acc);
        k2_mm(xf[1], wf[1], acc);
        STAMP(4);
#pragma unroll
        for (int m = 0; m < 4; ++m)
#pragma unroll
            for (int n = 0; n < 5; ++n) *(LAS f32x4*)(lds + w * 20480 + (m * 5 + n) * 1024 + lo) = acc[m][n] * gv[m] + accb[m][n];
    }
    LBAR();
    asm volatile(".Lgm_k2end:");
    STAMP(5);
#pragma unroll
    for (int i = 0; i < 3; ++i) {
        const int t = w + 8 * i;
        if (t < 20) {
            ldsp p = lds + t * 1024 + lo;
            const f32x4 y = ((*(const LAS f32x4*)(p) + *(const LAS f32x4*)(p + 20480)) + (*(const LAS f32x4*)(p + 40960) + *(const LAS f32x4*)(p + 61440)))
                          + ((*(const LAS f32x4*)(p + 81920) + *(const LAS f32x4*)(p + 102400)) + (*(const LAS f32x4*)(p + 122880) + *(const LAS f32x4*)(p + 143360)));
            const int m = t / 5, n = t - 5 * m;
            *(u32x2*)((char*)out + (size_t)(r0 + 32 * (m & 1) + 2 * l15 + (m >> 1)) * 1280 + ct * 160 + n * 32 + g * 8) = u32x2{pk2(y[0], y[1]), pk2(y[2], y[3])};
        }
    }
    STAMP_OUT(1);
}

extern "C" void kernel_launch(void* const* d_in, const int* in_sizes, int n_in,
                              void* d_out, int out_size, void* d_ws, size_t ws_size,
                              hipStream_t stream) {
    const __hip_bfloat16* x    = (const __hip_bfloat16*)d_in[0];
    const __hip_bfloat16* wA   = (const __hip_bfloat16*)d_in[1];
    const __hip_bfloat16* wphi = (const __hip_bfloat16*)d_in[2];
    const __hip_bfloat16* woA  = (const __hip_bfloat16*)d_in[3];
    const __hip_bfloat16* wB   = (const __hip_bfloat16*)d_in[5];
    const __hip_bfloat16* woB  = (const __hip_bfloat16*)d_in[6];
    const __hip_bfloat16* gw   = (const __hip_bfloat16*)d_in[7];
    char* ws = (char*)d_ws;
    k1_qkv<<<256, 512, 0, stream>>>(x, wA, wB, wphi, gw, woA, woB, ws, (const __hip_bfloat16*)d_out);
    k2_out<<<256, 512, 0, stream>>>(ws, (__hip_bfloat16*)d_out);
}
```

```cpp
#include <hip/hip_runtime.h>
#include <hip/hip_bf16.h>
#include <stdint.h>

typedef __bf16 bf16x8 __attribute__((ext_vector_type(8)));
typedef __bf16 bf16x2 __attribute__((ext_vector_type(2)));
typedef short s16x4 __attribute__((ext_vector_type(4)));
typedef float f32x2 __attribute__((ext_vector_type(2)));
typedef float f32x4 __attribute__((ext_vector_type(4)));
typedef float f32x16 __attribute__((ext_vector_type(16)));
typedef unsigned u32x2 __attribute__((ext_vector_type(2)));
typedef unsigned u32x4 __attribute__((ext_vector_type(4)));
#define LAS __attribute__((address_space(3)))
typedef LAS char* ldsp;

constexpr size_t WS_QF  = 0;
constexpr size_t WS_O   = WS_QF + 32ull * 64 * 1280;
constexpr size_t WS_KVP = WS_O + 32ull * 8 * 20480;
constexpr size_t WS_WOP = WS_KVP + 16ull * 32 * 840 * 4;
constexpr size_t WS_WPF = WS_WOP + 64ull * 25600;
constexpr size_t WS_G   = WS_WPF + 32ull * 64 * 32;
constexpr size_t WS_CNT = WS_G + 2048 * 4;

constexpr int ST_X = 144 * 128, ST_W = 3 * 64 * 160, ST = ST_X + ST_W;
constexpr int K1_LDS = 3 * ST;
constexpr int CSTR = 592;
constexpr int PHSTR = 328;
constexpr int L_PHIK = 144 * CSTR;
constexpr int L_GW = K1_LDS;
constexpr int L_WPH = L_GW + 2048;
constexpr int L_WO = L_WPH + 4096;
constexpr int K1_LDS_ALL = L_WO + 8192;
constexpr int L_FL = K1_LDS_ALL + 256;

#define SBAR() __builtin_amdgcn_sched_barrier(0)
#define LBAR() asm volatile("s_waitcnt lgkmcnt(0)\n\ts_barrier" ::: "memory")
#define MFMA16(a, b, c) __builtin_amdgcn_mfma_f32_16x16x32_bf16(a, b, c, 0, 0, 0)
#define MFMA32(a, b, c) __builtin_amdgcn_mfma_f32_32x32x16_bf16(a, b, c, 0, 0, 0)

__device__ __forceinline__ void glds16(const char* sbase, unsigned voff, unsigned lds_dst) {
    unsigned sv;
    asm volatile("s_mov_b32 %0, m0\n\ts_mov_b32 m0, %3\n\ts_nop 0\n\tglobal_load_lds_dwordx4 %1, %2\n\ts_mov_b32 m0, %0"
                 : "=&s"(sv) : "v"(voff), "s"(sbase), "s"(lds_dst) : "memory");
}
constexpr int PUBH = 6;
constexpr int SPL = 6;
#define K1AUX 16
#define WSRSRC(ws) __builtin_amdgcn_make_buffer_rsrc((void*)(ws), 0, (int)(WS_CNT + 4096), 0x00020000)
#define ST128(rs, off, v) __builtin_amdgcn_raw_buffer_store_b128((v), rs, (unsigned)(off), 0, K1AUX)
#define ST64(rs, off, v) __builtin_amdgcn_raw_buffer_store_b64((v), rs, (unsigned)(off), 0, K1AUX)
__device__ __forceinline__ bf16x8 lds_b128(ldsp p) { return *(const LAS bf16x8*)p; }
__device__ __forceinline__ u32x2 lds_b64(ldsp p) { return *(const LAS u32x2*)p; }
__device__ __forceinline__ u32x2 lds_tr(ldsp p) {
    return __builtin_bit_cast(u32x2, __builtin_amdgcn_ds_read_tr16_b64_v4i16((LAS s16x4*)p));
}
__device__ __forceinline__ bf16x8 cat2(u32x2 a, u32x2 b) {
    u32x4 r = {a[0], a[1], b[0], b[1]};
    return __builtin_bit_cast(bf16x8, r);
}
__device__ __forceinline__ unsigned pk2(float lo, float hi) {
    bf16x2 r = {(__bf16)lo, (__bf16)hi};
    return __builtin_bit_cast(unsigned, r);
}
__device__ __forceinline__ float rbf(float x) { return (float)(__bf16)x; }
__device__ __forceinline__ float bflo(unsigned u) { return __uint_as_float(u << 16); }
__device__ __forceinline__ float bfhi(unsigned u) { return __uint_as_float(u & 0xffff0000u); }
#define XHALF(v, A, B) do { auto r_ = __builtin_amdgcn_permlane32_swap(__float_as_uint(v), __float_as_uint(v), false, false); A = __uint_as_float(r_[0]); B = __uint_as_float(r_[1]); } while (0)
#define XQUAD(v, A, B) do { auto r_ = __builtin_amdgcn_permlane16_swap(__float_as_uint(v), __float_as_uint(v), false, false); A = __uint_as_float(r_[0]); B = __uint_as_float(r_[1]); } while (0)
__device__ __forceinline__ int krow_inv(int rho) {
    return (rho & 19) | ((rho & 4) << 1) | ((rho & 8) >> 1);
}
__device__ __forceinline__ int kstage(int s, int koff) {
    const int t = s + koff;
    return t >= 10 ? t - 10 : t;
}
constexpr float LOG2E = 1.4426950408889634f;

__device__ __forceinline__ void feat_map(const f32x16& p, unsigned (&o)[10]) {
    float mx = fmaxf(fmaxf(p[0], p[1]), p[2]), mn = fminf(fminf(p[0], p[1]), p[2]);
    mx = fmaxf(fmaxf(mx, p[3]), p[4]); mn = fminf(fminf(mn, p[3]), p[4]);
    mx = fmaxf(fmaxf(mx, p[5]), p[6]); mn = fminf(fminf(mn, p[5]), p[6]);
    mx = fmaxf(fmaxf(mx, p[7]), p[8]); mn = fminf(fminf(mn, p[7]), p[8]);
    mx = fmaxf(mx, p[9]); mn = fminf(mn, p[9]);
    float a, b;
    XHALF(mx, a, b); mx = fmaxf(a, b);
    XHALF(mn, a, b); mn = fminf(a, b);
    const f32x2 cp = {LOG2E, LOG2E}, cn = {-LOG2E, -LOG2E};
    const f32x2 mxl = {-mx * LOG2E, -mx * LOG2E}, mnl = {mn * LOG2E, mn * LOG2E};
    f32x2 ep[5], en[5];
#pragma unroll
    for (int i = 0; i < 5; ++i) {
        const f32x2 v = {p[2 * i], p[2 * i + 1]};
        const f32x2 ap = __builtin_elementwise_fma(v, cp, mxl), an = __builtin_elementwise_fma(v, cn, mnl);
        ep[i] = f32x2{__builtin_amdgcn_exp2f(ap[0]), __builtin_amdgcn_exp2f(ap[1])};
        en[i] = f32x2{__builtin_amdgcn_exp2f(an[0]), __builtin_amdgcn_exp2f(an[1])};
    }
    const f32x2 sp2 = ((ep[0] + ep[1]) + (ep[2] + ep[3])) + ep[4], sn2 = ((en[0] + en[1]) + (en[2] + en[3])) + en[4];
    float sp = sp2[0] + sp2[1], sn = sn2[0] + sn2[1];
    XHALF(sp, a, b); sp = a + b;
    XHALF(sn, a, b); sn = a + b;
    const float rp = __builtin_amdgcn_rcpf(sp), rn = __builtin_amdgcn_rcpf(sn);
    const f32x2 rp2 = {rp, rp}, rn2 = {rn, rn};
#pragma unroll
    for (int i = 0; i < 5; ++i) {
        const f32x2 tp = ep[i] * rp2, tn = en[i] * rn2;
        o[i] = pk2(tp[0], tp[1]);
        o[5 + i] = pk2(tn[0], tn[1]);
    }
}

__device__ __forceinline__ void pg_set(ldsp p, unsigned v) { *(volatile LAS unsigned*)p = v; }
__device__ __forceinline__ u32x4 pg_ld(ldsp p) { return *(volatile LAS u32x4*)p; }
__device__ __forceinline__ unsigned pg_min(u32x4 v) { return (unsigned)__builtin_amdgcn_readfirstlane(min(min(v[0], v[1]), min(v[2], v[3]))); }
__device__ __forceinline__ void pg_wait(ldsp p, unsigned need) {
    while (pg_min(pg_ld(p)) < need) __builtin_amdgcn_s_sleep(1);
    asm volatile("" ::: "memory");
}
template <int CW> struct K1Map {
    static constexpr int WM = CW >> 1, SIDE = CW & 1;
    static constexpr int nloc(int i) { return (WM == 1 && i < 5) ? (i + 3) % 5 : i; }
    static constexpr int ntile(int i) { return SIDE ? 14 - nloc(i) : nloc(i); }
    static constexpr int mtile(int a) { return 4 * WM + (SIDE ? ((a + 2) & 3) : a); }
    static constexpr int woff(int i) { return ST_X + (ntile(i) / 5) * 10240 + (ntile(i) % 5) * 32; }
};

template <int CW, bool ISB>
__device__ __forceinline__ void k1_compute(ldsp lds, int lane, int koff, float* gout) {
    using M = K1Map<CW>;
    constexpr bool GATE = !ISB;
    constexpr int NH = ISB ? 3 - M::WM : 0;
    const int g = lane >> 4, l15 = lane & 15;
    const unsigned xs0 = (unsigned)(l15 * 128 + ((g ^ (lane & 7)) * 16));
    const unsigned wb = (unsigned)((16 * (g >> 1) + 4 * (g & 1) + ((lane >> 2) & 3)) * 160 + (lane & 3) * 8);
    f32x4 acc[8][4], hacc[4];
#pragma unroll
    for (int i = 0; i < 8; ++i)
#pragma unroll
        for (int a = 0; a < 4; ++a) acc[i][a] = f32x4{0.f, 0.f, 0.f, 0.f};
#pragma unroll
    for (int i = 0; i < 4; ++i) hacc[i] = f32x4{0.f, 0.f, 0.f, 0.f};
    unsigned gk = (unsigned)(L_GW + kstage(0, koff) * 128 + g * 16);
    bf16x8 xf0[4], wf0[8], xh0, xf1[4], wf1[8], xh1;
#define RD(XF, WF, XH, BUF, HALF) do { \
        ldsp px = lds + (BUF) + (xs0 ^ ((HALF) * 64)); ldsp pw = lds + (BUF) + wb; \
        _Pragma("unroll") for (int a = 0; a < 4; ++a) XF[a] = lds_b128(px + (16 + 16 * M::mtile(a)) * 128); \
        if (ISB) XH = lds_b128(px); \
        if (GATE) XH = lds_b128(lds + gk + (HALF) * 64); \
        _Pragma("unroll") for (int i = 0; i < 8; ++i) \
            WF[i] = cat2(lds_tr(pw + M::woff(i) + (HALF) * 5120), lds_tr(pw + M::woff(i) + (HALF) * 5120 + 1280)); \
    } while (0)
#define MM(XF, WF, XH) MMR(XF, WF, XH, 0, 8)
#define MMR(XF, WF, XH, I0, I1) do { \
        _Pragma("unroll") for (int i = I0; i < I1; ++i) { \
            _Pragma("unroll") for (int a = 0; a < 4; ++a) { \
                if (i < 7 || a < 2) acc[i][a] = MFMA16(WF[i], XF[a], acc[i][a]); } \
            if (ISB && i < NH) hacc[i] = MFMA16(WF[i], XH, hacc[i]); \
            if (GATE && i < 2) hacc[i] = MFMA16(XH, XF[2 + i], hacc[i]); \
        } \
    } while (0)
#define ILV() do { \
        _Pragma("unroll") for (int t_ = 0; t_ < 21; ++t_) { \
            __builtin_amdgcn_sched_group_barrier(0x008, 1, 0); __builtin_amdgcn_sched_group_barrier(0x100, 1, 0); } \
        __builtin_amdgcn_sched_group_barrier(0x008, 24, 0); \
    } while (0)
    LBAR();
    pg_wait(lds + L_FL, 1u);
    unsigned cur = 0;
    RD(xf0, wf0, xh0, cur, 0);
    SBAR();
#pragma unroll 1
    for (int s = 0; s < 9; ++s) {
        RD(xf1, wf1, xh1, cur, 1);
        MMR(xf0, wf0, xh0, 0, SPL);
        ILV();
        SBAR();
        asm volatile("s_waitcnt lgkmcnt(0)" ::: "memory");
        pg_set(lds + L_FL + 16 + 4 * CW, (unsigned)(s + 1));
        const u32x4 fv = pg_ld(lds + L_FL);
        SBAR();
        MMR(xf0, wf0, xh0, SPL, 8);
        SBAR();
        cur = (cur == 2 * ST) ? 0u : cur + ST;
        if (GATE) gk = (unsigned)(L_GW + kstage(s + 1, koff) * 128 + g * 16);
        if (pg_min(fv) < (unsigned)(s + 2)) pg_wait(lds + L_FL, (unsigned)(s + 2));
        asm volatile("" ::: "memory");
        RD(xf0, wf0, xh0, cur, 0);
        MM(xf1, wf1, xh1);
        ILV();
        SBAR();
    }
    RD(xf1, wf1, xh1, cur, 1);
    MM(xf0, wf0, xh0);
    ILV();
    SBAR();
    MM(xf1, wf1, xh1);
#undef ILV
#undef RD
#undef MM
#undef MMR
    LBAR();
    ldsp cb = lds + l15 * CSTR;
#pragma unroll
    for (int i = 0; i < 8; ++i) {
        const int cq = 4 * M::ntile(i) + g;
        ldsp cc = cb + 8 * (cq + (cq * 205 >> 10));
#pragma unroll
        for (int a = 0; a < 4; ++a) {
            if (i < 7 || a < 2) {
                u32x2 v = {pk2(acc[i][a][0], acc[i][a][1]), pk2(acc[i][a][2], acc[i][a][3])};
                *(LAS u32x2*)(cc + (16 + 16 * M::mtile(a)) * CSTR) = v;
            }
        }
        if (ISB && i < NH) {
            u32x2 v = {pk2(hacc[i][0], hacc[i][1]), pk2(hacc[i][2], hacc[i][3])};
            *(LAS u32x2*)(cc) = v;
        }
    }
    if (GATE && gout != nullptr) {
        const float z = rbf((g & 1) ? hacc[1][0] : hacc[0][0]);
        if (lane < 32) __hip_atomic_store(gout + 16 * M::mtile(2) + lane, rbf(__builtin_amdgcn_rcpf(1.f + __builtin_amdgcn_exp2f(-z * LOG2E))), __ATOMIC_RELAXED, __HIP_MEMORY_SCOPE_AGENT);
    }
}

template <int V> struct IC { static constexpr int value = V; };
template <int LW>
__device__ __forceinline__ void k1_loader(ldsp lds, unsigned ldsb, int lane, const char* x, const char* wsrc, bool isB, int r0, int hg, int koff,
                                          const char* gatew, const char* wphi, const char* woA, const char* woB, int b, const __amdgpu_buffer_rsrc_t wrs) {
    if (LW < 2) { const unsigned c = 64u * LW + lane; glds16(gatew, (c < 80u ? c : 79u) * 16u, ldsb + L_GW + LW * 1024); }
    const unsigned xo = (unsigned)((lane >> 3) * 1280 + (((lane & 7) ^ (lane >> 3)) * 16));
    const char* xb = x + (size_t)(r0 - 16) * 1280;
    const char* xh = x + (size_t)(r0 >= 16 ? r0 - 16 : 0) * 1280;
    unsigned wo[5];
    if (LW > 0) {
#pragma unroll
        for (int q = 0; q < 5; ++q) {
            const unsigned c = 64u * q + lane;
            const unsigned rho = (c * 205u) >> 11, ch = c - rho * 10u;
            wo[q] = (unsigned)krow_inv((int)rho) * 3840u + ch * 16u;
        }
    }
    const char* wbase = wsrc + hg * 160;
    const int sc0 = isB ? 1280 : 0, sc1 = isB ? 0 : 1280;
    auto issue = [&](int s, unsigned buf, auto I0, auto I1) {
        const int ks = kstage(s, koff);
#pragma unroll
        for (int i = I0.value; i < I1.value; ++i) {
            const int p = 12 * LW + i;
            if (p < 18) {
                const char* sb = (p < 2 ? xh : xb) + ks * 128 + p * 10240;
                if (p >= 2 || isB) glds16(sb, xo, ldsb + buf + p * 1024);
            } else {
                const int q = p - 18, seg = q / 10, hf = (q % 10) / 5;
                const char* sb = wbase + (size_t)ks * (64 * 3840) + hf * (32 * 3840) + (seg == 0 ? sc0 : (seg == 1 ? sc1 : 2560));
                glds16(sb, wo[q % 5], ldsb + buf + p * 1024);
            }
        }
    };
    unsigned cur = 0;
#pragma unroll 1
    for (int s = 0; s < 10; ++s) {
        if (s >= 3) pg_wait(lds + L_FL + 16, (unsigned)(s - 2));
        issue(s, cur, IC<0>{}, IC<PUBH>{});
        if (s > 0) {
            if (LW == 0 && !isB) asm volatile("s_waitcnt vmcnt(%0)" :: "i"(PUBH - 2) : "memory"); else asm volatile("s_waitcnt vmcnt(%0)" :: "i"(PUBH) : "memory");
            pg_set(lds + L_FL + 4 * LW, (unsigned)s);
        }
        issue(s, cur, IC<PUBH>{}, IC<12>{});
        cur = (cur == 2 * ST) ? 0u : cur + ST;
        if (s == 0) asm volatile("s_barrier" ::: "memory");
        if (s == 3) {
            const unsigned c = 64u * LW + lane;
            glds16(wphi + hg * 3200, (c < 200u ? c : 199u) * 16u, ldsb + L_WPH + LW * 1024);
            const int bb = b < 200 ? b : 199;
            const int kb = bb / 5, ng = bb - kb * 5, st = kb / 5, j = kb - st * 5;
            const char* wo = ((st >> 2) ? woA : woB) + (size_t)(8 * (st & 3)) * 20 * 1280 + ng * 256;
#pragma unroll
            for (int i = 0; i < 2; ++i) {
                const int kk = 8 * LW + 4 * i + (lane >> 4), e = kk & 7;
                const int head = j < 4 ? 2 * j + (LW >> 1) : 2 * LW + (e >> 2);
                const int d = j < 4 ? 8 * (LW & 1) + e : 16 + (e & 3);
                glds16(wo, (unsigned)((head * 20 + d) * 1280 + (lane & 15) * 16), ldsb + L_WO + (2 * LW + i) * 1024);
            }
        }
    }
}

__global__ __launch_bounds__(512, 2) void k1_qkv(const __hip_bfloat16* __restrict__ x, const __hip_bfloat16* __restrict__ wA,
                                                   const __hip_bfloat16* __restrict__ wB, const __hip_bfloat16* __restrict__ wphi,
                                                   const __hip_bfloat16* __restrict__ gatew, const __hip_bfloat16* __restrict__ woA,
                                                   const __hip_bfloat16* __restrict__ woB, char* __restrict__ ws) {
    __shared__ __attribute__((aligned(16))) char smem[K1_LDS_ALL + 256 + 128];
    ldsp lds = (ldsp)smem;
    const int tid = threadIdx.x, lane = tid & 63;
    const int w = __builtin_amdgcn_readfirstlane(tid >> 6);
    if (w == 0) {
        unsigned long long pc0; unsigned sv;
        asm volatile("s_getpc_b64 %1\n\ts_mov_b32 %0, m0\n\ts_mov_b32 m0, %3\n\ts_nop 0\n\tglobal_load_lds_dword %2, %1\n\ts_mov_b32 m0, %0"
                     : "=&s"(sv), "=&s"(pc0) : "v"(lane * 128), "s"((unsigned)(uintptr_t)lds + K1_LDS_ALL) : "memory");
    }
    asm volatile("" :: "s"(x), "s"(wA), "s"(wB), "s"(wphi), "s"(gatew), "s"(woA), "s"(woB), "s"(ws));
    const int b = blockIdx.x, xcd = b & 7, idx = b >> 3;
    const int rt = 8 * (xcd & 1) + (idx & 7);
    const int cg = 4 * (xcd >> 1) + (idx >> 3);
    const bool isB = cg >= 8;
    const int hg = cg & 7;
    const int r0 = rt * 128;
    const int r32 = lane & 31, hh = lane >> 5;
    const __amdgpu_buffer_rsrc_t wrs = WSRSRC(ws);
    const int koff = (3 * (idx >> 3) + (idx & 7)) % 10;

    if (w < 4) {
        {
            unsigned long long pc; int len;
            asm volatile("s_getpc_b64 %0\n.Lgm_here:\n\ts_mov_b32 %1, .Lgm_k2end - .Lgm_here" : "=s"(pc), "=s"(len));
            const int off = min(idx * 2048 + w * 512 + (lane & 3) * 128, len - 4);
            if (len > 4) {
                unsigned sv;
                asm volatile("s_mov_b32 %0, m0\n\ts_mov_b32 m0, %3\n\ts_nop 0\n\tglobal_load_lds_dword %1, %2\n\ts_mov_b32 m0, %0"
                             : "=&s"(sv) : "v"(off), "s"(pc), "s"((unsigned)(uintptr_t)lds + K1_LDS_ALL) : "memory");
            }
        }
        if (tid < 32) *(volatile LAS unsigned*)(lds + L_FL + tid * 4) = 0u;
        float* gout = cg == 0 ? (float*)(ws + WS_G) + r0 : nullptr;
        if (isB) {
            if (w == 0) k1_compute<0, true>(lds, lane, koff, gout); else if (w == 1) k1_compute<1, true>(lds, lane, koff, gout);
            else if (w == 2) k1_compute<2, true>(lds, lane, koff, gout); else k1_compute<3, true>(lds, lane, koff, gout);
        } else {
            if (w == 0) k1_compute<0, false>(lds, lane, koff, gout); else if (w == 1) k1_compute<1, false>(lds, lane, koff, gout);
            else if (w == 2) k1_compute<2, false>(lds, lane, koff, gout); else k1_compute<3, false>(lds, lane, koff, gout);
        }
    } else {
        const unsigned ldsb = (unsigned)(uintptr_t)lds;
        const char* wsrc = (const char*)(isB ? wB : wA);
        if (w == 4) k1_loader<0>(lds, ldsb, lane, (const char*)x, wsrc, isB, r0, hg, koff, (const char*)gatew, (const char*)wphi, (const char*)woA, (const char*)woB, b, wrs);
        else if (w == 5) k1_loader<1>(lds, ldsb, lane, (const char*)x, wsrc, isB, r0, hg, koff, (const char*)gatew, (const char*)wphi, (const char*)woA, (const char*)woB, b, wrs);
        else if (w == 6) k1_loader<2>(lds, ldsb, lane, (const char*)x, wsrc, isB, r0, hg, koff, (const char*)gatew, (const char*)wphi, (const char*)woA, (const char*)woB, b, wrs);
        else k1_loader<3>(lds, ldsb, lane, (const char*)x, wsrc, isB, r0, hg, koff, (const char*)gatew, (const char*)wphi, (const char*)woA, (const char*)woB, b, wrs);
        asm volatile("s_waitcnt vmcnt(0)" ::: "memory");
        pg_set(lds + L_FL + 4 * (w - 4), 10u);
        pg_wait(lds + L_FL, 6u);
        const int lw = w - 4;
        u32x4 wof[2];
#pragma unroll
        for (int i = 0; i < 2; ++i) {
            ldsp pw = lds + L_WO + (8 * (lane >> 4) + ((lane >> 2) & 3)) * 256 + (2 * lw + i) * 32 + (lane & 3) * 8;
            const u32x2 f0 = lds_tr(pw), f1 = lds_tr(pw + 1024);
            wof[i] = u32x4{f0[0], f0[1], f1[0], f1[1]};
        }
        u32x4 t0, t1;
        {
            const int rho = (r32 & 3) + 4 * (r32 >> 3), ho = (r32 >> 2) & 1;
            const bool vr = rho < 10, v1 = vr && hh == 0;
            const LAS unsigned short* wpp = (const LAS unsigned short*)(lds + L_WPH + lw * 800) + (vr ? 10 * ho + rho : 0);
#pragma unroll
            for (int j = 0; j < 4; ++j) { const unsigned v = (unsigned)wpp[(8 * hh + 2 * j) * 20] | ((unsigned)wpp[(8 * hh + 2 * j + 1) * 20] << 16); t0[j] = vr ? v : 0u; }
#pragma unroll
            for (int j = 0; j < 2; ++j) { const unsigned v = (unsigned)wpp[(16 + 2 * j) * 20] | ((unsigned)wpp[(17 + 2 * j) * 20] << 16); t1[j] = v1 ? v : 0u; }
            t1[2] = 0u; t1[3] = 0u;
        }
        {
            const u32x4 sent = {0xffffffffu, 0xffffffffu, 0xffffffffu, 0xffffffffu};
            const unsigned dm = (unsigned)WS_O + ((b >> 3) * 8 + 4 + ((b >> 1) & 3)) * 20480 + (b & 1) * 10240 + lw * 1024 + lane * 16;
            ST128(wrs, dm, sent); ST128(wrs, dm + 4096, sent);
            if (lw < 2) ST128(wrs, dm + 8192, sent);
        }
        if (b < 200) {
            const int kb = b / 5, ng = b - kb * 5, st = kb / 5, j = kb - st * 5;
#pragma unroll
            for (int i = 0; i < 2; ++i) {
                const int nt = 8 * ng + 2 * lw + i, ct = nt / 5, n = nt - ct * 5;
                ST128(wrs, (unsigned)WS_WOP + (((ct * 8 + st) * 5 + j) * 5 + n) * 1024 + lane * 16, wof[i]);
            }
        }
        if (!isB && rt == 0) {
            const unsigned dst = (unsigned)WS_WPF + (4 * hg + lw) * 2048 + lane * 16;
            ST128(wrs, dst, t0);
            ST128(wrs, dst + 1024, t1);
        }
        LBAR();
        *(LAS u32x4*)(lds + L_WO + lw * 1024 + lane * 16) = t0;
        *(LAS u32x2*)(lds + L_WO + 4096 + lw * 512 + lane * 8) = u32x2{t1[0], t1[1]};
    }
    LBAR();
    if (!isB) {
        const int hl = w & 3, rp = w >> 2;
        const int h = 4 * hg + hl;
        const u32x2 z2 = {0u, 0u};
        const bf16x8 wa0 = lds_b128(lds + L_WO + hl * 1024 + lane * 16);
        const bf16x8 wa1 = cat2(lds_b64(lds + L_WO + 4096 + hl * 512 + lane * 8), z2);
        f32x16 p[2];
#pragma unroll
        for (int u = 0; u < 2; ++u) {
            ldsp cp = lds + (16 + 32 * (2 * rp + u) + r32) * CSTR + 192 + 48 * hl;
            bf16x8 b0 = lds_b128(cp + hh * 16);
            u32x2 t = lds_b64(cp + 32);
            if (hh) t = z2;
            p[u] = MFMA32(wa0, b0, (f32x16{}));
            p[u] = MFMA32(wa1, cat2(t, z2), p[u]);
        }
#pragma unroll
        for (int u = 0; u < 2; ++u) {
            ldsp cp = lds + (16 + 32 * (2 * rp + u) + r32) * CSTR + 48 * hl;
            const u32x4 a0 = *(const LAS u32x4*)(cp + hh * 16); const u32x2 t = lds_b64(cp + 32);
            const unsigned dst = (unsigned)WS_QF + (h * 64 + (rt * 4 + 2 * rp + u)) * 1280;
            ST128(wrs, dst + lane * 16, a0);
            if (hh == 0) ST64(wrs, dst + 1024 + r32 * 8, t);
        }
#pragma unroll
        for (int u = 0; u < 2; ++u) {
            unsigned o[10];
            feat_map(p[u], o);
            ldsp pp = lds + L_PHIK + (32 * (2 * rp + u) + r32) * PHSTR + hl * 80 + hh * 40;
#pragma unroll
            for (int i = 0; i < 5; ++i) *(LAS u32x2*)(pp + 8 * i) = u32x2{o[2 * i], o[2 * i + 1]};
        }
        LBAR();
        {
            const int nt = w >> 2;
            const int g = lane >> 4, q = (lane >> 2) & 3, pp = lane & 3, l15 = lane & 15;
            const int rowl = 4 * (4 * (g & 1) + q) + 2 * (g >> 1);
            ldsp pa = lds + L_PHIK + rowl * PHSTR + hl * 80 + pp * 8;
            ldsp pv = lds + (16 + rowl) * CSTR + 384 + 48 * hl + 32 * nt + 8 * pp;
            f32x4 kv[3];
#pragma unroll
            for (int m = 0; m < 3; ++m) kv[m] = f32x4{0.f, 0.f, 0.f, 0.f};
            const bool ones = (nt == 1) && (l15 == 4);
#pragma unroll
            for (int kk = 0; kk < 4; ++kk) {
                u32x2 v0 = lds_tr(pv + kk * 32 * CSTR), v1 = lds_tr(pv + kk * 32 * CSTR + CSTR);
                if (ones) { v0 = u32x2{0x3f803f80u, 0x3f803f80u}; v1 = v0; }
                bf16x8 vb = cat2(v0, v1);
#pragma unroll
                for (int m = 0; m < 3; ++m) {
                    bf16x8 af = cat2(lds_tr(pa + kk * 32 * PHSTR + m * 32), lds_tr(pa + kk * 32 * PHSTR + PHSTR + m * 32));
                    kv[m] = MFMA16(af, vb, kv[m]);
                }
            }
            const int d = 16 * nt + l15;
            const unsigned dst = (unsigned)WS_KVP + ((rt * 32 + h) * 840 + d * 40 + 4 * g) * 2;
            if (d < 21) {
                ST64(wrs, dst, (u32x2{pk2(kv[0][0], kv[0][1]), pk2(kv[0][2], kv[0][3])}));
                ST64(wrs, dst + 32, (u32x2{pk2(kv[1][0], kv[1][1]), pk2(kv[1][2], kv[1][3])}));
                if (g < 2) ST64(wrs, dst + 64, (u32x2{pk2(kv[2][0], kv[2][1]), pk2(kv[2][2], kv[2][3])}));
            }
        }
    } else {
        const int hl = w & 3, par = w >> 2;
        const int g = lane >> 4, l15 = lane & 15;
        const u32x2 z2 = {0u, 0u};
        const int gc = g < 2 ? g : 2;
        ldsp kq = lds + (16 + 2 * l15 + par) * CSTR + 48 * hl + 16 * gc;
        bf16x8 kf[5], qf[4];
        auto fixk = [&](u32x4 v) { if (g >= 2) { v[2] = 0u; v[3] = 0u; } if (g == 3) { v[0] = 0u; v[1] = 0u; } return __builtin_bit_cast(bf16x8, v); };
        kf[0] = fixk(*(const LAS u32x4*)(lds + (l15 >= 8 ? 2 * l15 - 16 + par : par) * CSTR + 48 * hl + 16 * gc));
#pragma unroll
        for (int kb = 0; kb < 4; ++kb) {
            kf[kb + 1] = fixk(*(const LAS u32x4*)(kq + kb * 32 * CSTR));
            qf[kb] = fixk(*(const LAS u32x4*)(kq + 192 + kb * 32 * CSTR));
        }
        u32x2 vt[5][2];
        {
            const int q4 = (lane >> 2) & 3, pp = lane & 3, key = 4 * g + q4;
            ldsp vp = lds + (16 + 2 * key + par) * CSTR + 384 + 48 * hl + 8 * pp;
            ldsp v0 = lds + (key >= 8 ? 2 * key - 16 + par : par) * CSTR + 384 + 48 * hl + 8 * pp;
            vt[0][0] = lds_tr(v0); vt[0][1] = lds_tr(v0 + 32);
#pragma unroll
            for (int kb = 0; kb < 4; ++kb) { vt[kb + 1][0] = lds_tr(vp + kb * 32 * CSTR); vt[kb + 1][1] = lds_tr(vp + kb * 32 * CSTR + 32); }
        }
        const float NEG = -__builtin_inff();
        float b0[4], b1[4];
#pragma unroll
        for (int e = 0; e < 4; ++e) {
            const int t = 4 * g + e;
            b0[e] = (t >= l15 + 9) ? 0.f : NEG;
            b1[e] = (t <= l15 && t >= l15 - 7) ? 0.f : NEG;
        }
        const float sc = 0.22360679774997896f * LOG2E;
        const int hB = 4 * hg + hl;
        const f32x4 zero4 = {0.f, 0.f, 0.f, 0.f};
        f32x4 s0[4], s1[4];
#pragma unroll
        for (int qb = 0; qb < 4; ++qb) { s0[qb] = MFMA16(kf[qb], qf[qb], zero4); s1[qb] = MFMA16(kf[qb + 1], qf[qb], zero4); }
        float mx[4], sum[4];
#pragma unroll
        for (int qb = 0; qb < 4; ++qb) {
#pragma unroll
            for (int e = 0; e < 4; ++e) {
                s0[qb][e] = fmaf(s0[qb][e], sc, (qb == 0 && rt == 0) ? NEG : b0[e]);
                s1[qb][e] = fmaf(s1[qb][e], sc, b1[e]);
            }
            mx[qb] = fmaxf(fmaxf(fmaxf(s0[qb][0], s0[qb][1]), fmaxf(s0[qb][2], s0[qb][3])), fmaxf(fmaxf(s1[qb][0], s1[qb][1]), fmaxf(s1[qb][2], s1[qb][3])));
        }
#pragma unroll
        for (int qb = 0; qb < 4; ++qb) { float a, bb; XQUAD(mx[qb], a, bb); mx[qb] = fmaxf(a, bb); }
#pragma unroll
        for (int qb = 0; qb < 4; ++qb) { float a, bb; XHALF(mx[qb], a, bb); mx[qb] = fmaxf(a, bb); }
#pragma unroll
        for (int qb = 0; qb < 4; ++qb) {
#pragma unroll
            for (int e = 0; e < 4; ++e) { s0[qb][e] = __builtin_amdgcn_exp2f(s0[qb][e] - mx[qb]); s1[qb][e] = __builtin_amdgcn_exp2f(s1[qb][e] - mx[qb]); }
            const f32x4 t = s0[qb] + s1[qb];
            sum[qb] = (t[0] + t[1]) + (t[2] + t[3]);
        }
#pragma unroll
        for (int qb = 0; qb < 4; ++qb) { float a, bb; XQUAD(sum[qb], a, bb); sum[qb] = a + bb; }
#pragma unroll
        for (int qb = 0; qb < 4; ++qb) { float a, bb; XHALF(sum[qb], a, bb); sum[qb] = __builtin_amdgcn_rcpf(a + bb); }
        u32x2 om[4], ot[4];
#pragma unroll
        for (int qb = 0; qb < 4; ++qb) {
            const f32x4 p0 = s0[qb] * sum[qb], p1 = s1[qb] * sum[qb];
            const bf16x8 pf = cat2(u32x2{pk2(p0[0], p0[1]), pk2(p0[2], p0[3])}, u32x2{pk2(p1[0], p1[1]), pk2(p1[2], p1[3])});
            const f32x4 o0 = MFMA16(cat2(vt[qb][0], vt[qb + 1][0]), pf, zero4);
            const f32x4 o1 = MFMA16(cat2(vt[qb][1], vt[qb + 1][1]), pf, zero4);
            om[qb] = u32x2{pk2(o0[0], o0[1]), pk2(o0[2], o0[3])};
            ot[qb] = u32x2{pk2(o1[0], o1[1]), pk2(o1[2], o1[3])};
        }
        const unsigned ob = (unsigned)WS_O + (2 * rt * 8 + (hB >> 3)) * 20480;
        const unsigned slot = (unsigned)(par * 32 + l15);
#pragma unroll
        for (int qb = 0; qb < 4; ++qb) ST64(wrs, ob + (qb >> 1) * (8 * 20480) + (hB & 7) * 2048 + (slot + 16 * (qb & 1)) * 32 + g * 8, om[qb]);
        if (g == 0) {
#pragma unroll
            for (int qb = 0; qb < 4; ++qb) ST64(wrs, ob + (qb >> 1) * (8 * 20480) + 16384 + (hB & 7) * 512 + (slot + 16 * (qb & 1)) * 8, ot[qb]);
        }
    }
}

constexpr int K2_LDS = 8 * 20480;
constexpr int K2_KV = 4 * 20480;
constexpr int K2_FL = K2_KV + 1792;
__device__ __forceinline__ u32x4 bld128(__amdgpu_buffer_rsrc_t rs, unsigned voff, unsigned soff, bool sc1) {
    return sc1 ? __builtin_amdgcn_raw_buffer_load_b128(rs, voff, soff, 16) : __builtin_amdgcn_raw_buffer_load_b128(rs, voff, soff, 0);
}
template <bool SC1>
__device__ __forceinline__ void k2_ldx(__amdgpu_buffer_rsrc_t ro, unsigned xm, unsigned xt, int j, u32x4 (&xf)[4]) {
#pragma unroll
    for (int m = 0; m < 4; ++m) {
        if (j < 4) xf[m] = bld128(ro, xm + m * 512, j * 4096, SC1);
        else {
            const u32x2 t0 = SC1 ? __builtin_amdgcn_raw_buffer_load_b64(ro, xt + m * 128, 16384, 16) : __builtin_amdgcn_raw_buffer_load_b64(ro, xt + m * 128, 16384, 0);
            const u32x2 t1 = SC1 ? __builtin_amdgcn_raw_buffer_load_b64(ro, xt + m * 128 + 512, 16384, 16) : __builtin_amdgcn_raw_buffer_load_b64(ro, xt + m * 128 + 512, 16384, 0);
            xf[m] = u32x4{t0[0], t0[1], t1[0], t1[1]};
        }
    }
}
__device__ __forceinline__ void k2_ldw(__amdgpu_buffer_rsrc_t rw, unsigned lo, int j, u32x4 (&wf)[5]) {
#pragma unroll
    for (int n = 0; n < 5; ++n) wf[n] = bld128(rw, lo + (n & 3) * 1024, j * 5120 + (n >> 2) * 4096, false);
}
__device__ __forceinline__ void k2_mm(const u32x4 (&xf)[4], const u32x4 (&wf)[5], f32x4 (&acc)[4][5]) {
#pragma unroll
    for (int m = 0; m < 4; ++m)
#pragma unroll
        for (int n = 0; n < 5; ++n) acc[m][n] = MFMA16(__builtin_bit_cast(bf16x8, wf[n]), __builtin_bit_cast(bf16x8, xf[m]), acc[m][n]);
}
__device__ __forceinline__ unsigned k2_max(const u32x4 (&xf)[4], unsigned mx) {
#pragma unroll
    for (int m = 0; m < 4; ++m) { mx = max(max(mx, xf[m][0]), xf[m][1]); mx = max(max(mx, xf[m][2]), xf[m][3]); }
    return mx;
}

__global__ __launch_bounds__(512, 2) void k2_out(char* __restrict__ ws, __hip_bfloat16* __restrict__ out) {
    __shared__ __attribute__((aligned(16))) char smem[K2_LDS];
    ldsp lds = (ldsp)smem;
    const int tid = threadIdx.x, lane = tid & 63;
    const int w = __builtin_amdgcn_readfirstlane(tid >> 6);
    const int b = blockIdx.x, xcd = b & 7, idx = b >> 3;
    const int rt = 8 * (xcd & 3) + (idx & 7);
    const int ct = 4 * (xcd >> 2) + (idx >> 3);
    const int r0 = rt * 64;
    const int g = lane >> 4, l15 = lane & 15, r32 = lane & 31, hh = lane >> 5;
    const u32x2 z2 = {0u, 0u};
    const __amdgpu_buffer_rsrc_t ro = __builtin_amdgcn_make_buffer_rsrc(ws + WS_O + (size_t)(rt * 8 + w) * 20480, 0, 20480, 0x00020000);
    const __amdgpu_buffer_rsrc_t rw = __builtin_amdgcn_make_buffer_rsrc(ws + WS_WOP + (size_t)(ct * 8 + w) * 25600, 0, 25600, 0x00020000);
    const unsigned xm = (unsigned)((g >> 1) * 2048 + l15 * 32 + (g & 1) * 16);
    const unsigned xt = (unsigned)(2 * g * 512 + l15 * 8);
    const unsigned lo = (unsigned)lane * 16u;
    f32x4 acc[4][5];
#pragma unroll
    for (int m = 0; m < 4; ++m)
#pragma unroll
        for (int n = 0; n < 5; ++n) acc[m][n] = f32x4{0.f, 0.f, 0.f, 0.f};
    float gv[4];

    if (w >= 4) {
        const int pw = w - 4, pt = tid - 256;
        const int h = b & 31, c = b >> 5;
        u32x4 wf[3][5], xf[3][4];
        {
            u32x4 q0[2]; u32x2 q1[2];
#pragma unroll
            for (int u = 0; u < 2; ++u) {
                const char* pq = ws + WS_QF + ((size_t)h * 64 + c * 8 + 2 * pw + u) * 1280;
                q0[u] = *(const u32x4*)(pq + lane * 16);
                q1[u] = *(const u32x2*)(pq + 1024 + r32 * 8);
            }
            const u32x4 wa0 = *(const u32x4*)(ws + WS_WPF + (size_t)h * 2048 + lane * 16);
            const u32x4 wa1 = *(const u32x4*)(ws + WS_WPF + (size_t)h * 2048 + 1024 + lane * 16);
            u32x2 t[16];
            {
                const int tc = pt < 210 ? pt : 209;
                const char* kp = ws + WS_KVP + (size_t)h * 1680 + tc * 8;
#pragma unroll
                for (int r = 0; r < 16; ++r) t[r] = *(const u32x2*)(kp + (size_t)r * (32 * 1680));
            }
            if (pt < 2) *(volatile LAS unsigned*)(lds + K2_FL + 4 * pt) = 0u;
            LBAR();
            unsigned ph[2][10];
#pragma unroll
            for (int u = 0; u < 2; ++u) {
                u32x2 tq = q1[u]; if (hh) tq = z2;
                f32x16 p = MFMA32(__builtin_bit_cast(bf16x8, wa0), __builtin_bit_cast(bf16x8, q0[u]), (f32x16{}));
                p = MFMA32(__builtin_bit_cast(bf16x8, wa1), cat2(tq, z2), p);
                feat_map(p, ph[u]);
            }
            SBAR();
            f32x4 ks = {0.f, 0.f, 0.f, 0.f};
#pragma unroll
            for (int r = 0; r < 16; ++r) ks += f32x4{bflo(t[r][0]), bfhi(t[r][0]), bflo(t[r][1]), bfhi(t[r][1])};
            if (pt < 210) *(LAS u32x2*)(lds + K2_KV + pt * 8) = u32x2{pk2(ks[0], ks[1]), pk2(ks[2], ks[3])};
            asm volatile("s_waitcnt lgkmcnt(0)" ::: "memory");
            if (lane == 0) __hip_atomic_fetch_add((LAS unsigned*)(lds + K2_FL), 1u, __ATOMIC_RELAXED, __HIP_MEMORY_SCOPE_WORKGROUP);
            while ((unsigned)__builtin_amdgcn_readfirstlane(__hip_atomic_load((LAS unsigned*)(lds + K2_FL), __ATOMIC_RELAXED, __HIP_MEMORY_SCOPE_WORKGROUP)) < 4u) __builtin_amdgcn_s_sleep(1);
            asm volatile("" ::: "memory");
            const int rho = (r32 & 3) + 4 * (r32 >> 3), ho = (r32 >> 2) & 1;
            const bool vr = rho <= 10;
            const int d = rho < 8 ? 8 * ho + rho : (rho < 10 ? 16 + 2 * ho + (rho - 8) : 20);
            ldsp ap = lds + K2_KV + d * 80 + hh * 40;
            u32x2 a00 = lds_b64(ap), a01 = lds_b64(ap + 8), a10 = lds_b64(ap + 16), a11 = lds_b64(ap + 24), a20 = lds_b64(ap + 32);
            asm volatile("s_waitcnt lgkmcnt(0)" ::: "memory");
            if (lane == 0) __hip_atomic_fetch_add((LAS unsigned*)(lds + K2_FL + 4), 1u, __ATOMIC_RELAXED, __HIP_MEMORY_SCOPE_WORKGROUP);
            if (!vr) { a00 = z2; a01 = z2; a10 = z2; a11 = z2; a20 = z2; }
            const __amdgpu_buffer_rsrc_t rs = __builtin_amdgcn_make_buffer_rsrc(ws + WS_O + (size_t)((c * 4 + pw) * 8 + 4 + (h >> 3)) * 20480, 0, 20480, 0x00020000);
#pragma unroll
            for (int u = 0; u < 2; ++u) {
                f32x16 p = MFMA32(cat2(a00, a01), cat2(u32x2{ph[u][0], ph[u][1]}, u32x2{ph[u][2], ph[u][3]}), (f32x16{}));
                p = MFMA32(cat2(a10, a11), cat2(u32x2{ph[u][4], ph[u][5]}, u32x2{ph[u][6], ph[u][7]}), p);
                p = MFMA32(cat2(a20, z2), cat2(u32x2{ph[u][8], ph[u][9]}, z2), p);
                const float rd = __builtin_amdgcn_rcpf(p[10] + 1e-6f);
                const int srow = (r32 & 1) * 32 + u * 16 + (r32 >> 1);
                u32x4 m = {pk2(p[0] * rd, p[1] * rd), pk2(p[2] * rd, p[3] * rd), pk2(p[4] * rd, p[5] * rd), pk2(p[6] * rd, p[7] * rd)};
                __builtin_amdgcn_raw_buffer_store_b128(m, rs, (unsigned)((h & 7) * 2048 + srow * 32 + hh * 16), 0, 16);
                __builtin_amdgcn_raw_buffer_store_b32(pk2(p[8] * rd, p[9] * rd), rs, (unsigned)(16384 + (h & 7) * 512 + srow * 8 + hh * 4), 0, 16);
            }
        }
        SBAR();
#pragma unroll
        for (int j = 0; j < 3; ++j) k2_ldw(rw, lo, j, wf[j]);
#pragma unroll
        for (int m = 0; m < 4; ++m) gv[m] = ((const float*)(ws + WS_G))[r0 + 32 * (m & 1) + 2 * l15 + (m >> 1)];
        SBAR();
        int redo = 0;
#pragma unroll 1
        for (;;) {
            asm volatile("" ::: "memory");
#pragma unroll
            for (int j = 0; j < 3; ++j) k2_ldx<true>(ro, xm, xt, j, xf[j]);
            unsigned mx = 0u;
#pragma unroll
            for (int j = 0; j < 3; ++j) mx = k2_max(xf[j], mx);
            if (__builtin_amdgcn_ballot_w64(mx == 0xffffffffu) == 0ull || ++redo > (1 << 16)) break;
            __builtin_amdgcn_s_sleep(2);
        }
#pragma unroll
        for (int j = 0; j < 3; ++j) k2_mm(xf[j], wf[j], acc);
        if (w == 4) {
            while ((unsigned)__builtin_amdgcn_readfirstlane(__hip_atomic_load((LAS unsigned*)(lds + K2_FL + 4), __ATOMIC_RELAXED, __HIP_MEMORY_SCOPE_WORKGROUP)) < 4u) __builtin_amdgcn_s_sleep(1);
            asm volatile("" ::: "memory");
        }
#pragma unroll
        for (int m = 0; m < 4; ++m)
#pragma unroll
            for (int n = 0; n < 5; ++n) *(LAS f32x4*)(lds + w * 20480 + (m * 5 + n) * 1024 + lo) = acc[m][n] * gv[m];
    } else {
        asm volatile("s_barrier" ::: "memory");
        {
            u32x4 wf[5][5], xf[5][4];
#pragma unroll
            for (int j = 0; j < 3; ++j) { k2_ldx<false>(ro, xm, xt, j, xf[j]); k2_ldw(rw, lo, j, wf[j]); }
#pragma unroll
            for (int m = 0; m < 4; ++m) gv[m] = ((const float*)(ws + WS_G))[r0 + 32 * (m & 1) + 2 * l15 + (m >> 1)];
            SBAR();
            k2_mm(xf[0], wf[0], acc);
            SBAR();
            k2_ldx<false>(ro, xm, xt, 3, xf[3]); k2_ldw(rw, lo, 3, wf[3]);
            SBAR();
            k2_mm(xf[1], wf[1], acc);
            SBAR();
            k2_ldx<false>(ro, xm, xt, 4, xf[4]); k2_ldw(rw, lo, 4, wf[4]);
            SBAR();
            k2_mm(xf[2], wf[2], acc);
            k2_mm(xf[3], wf[3], acc);
            k2_mm(xf[4], wf[4], acc);
        }
        SBAR();
        const __amdgpu_buffer_rsrc_t ro2 = __builtin_amdgcn_make_buffer_rsrc(ws + WS_O + (size_t)(rt * 8 + w + 4) * 20480, 0, 20480, 0x00020000);
        const __amdgpu_buffer_rsrc_t rw2 = __builtin_amdgcn_make_buffer_rsrc(ws + WS_WOP + (size_t)(ct * 8 + w + 4) * 25600, 0, 25600, 0x00020000);
        u32x4 wf[2][5], xf[2][4];
        k2_ldw(rw2, lo, 3, wf[0]); k2_ldw(rw2, lo, 4, wf[1]);
        f32x4 accb[4][5];
#pragma unroll
        for (int m = 0; m < 4; ++m) {
            const float gb = rbf(1.f - gv[m]);
#pragma unroll
            for (int n = 0; n < 5; ++n) { accb[m][n] = acc[m][n] * gb; acc[m][n] = f32x4{0.f, 0.f, 0.f, 0.f}; }
        }
        SBAR();
        int redo = 0;
#pragma unroll 1
        for (;;) {
            asm volatile("" ::: "memory");
            k2_ldx<true>(ro2, xm, xt, 3, xf[0]); k2_ldx<true>(ro2, xm, xt, 4, xf[1]);
            const unsigned mx = k2_max(xf[1], k2_max(xf[0], 0u));
            if (__builtin_amdgcn_ballot_w64(mx == 0xffffffffu) == 0ull || ++redo > (1 << 16)) break;
            __builtin_amdgcn_s_sleep(2);
        }
        k2_mm(xf[0], wf[0], acc);
        k2_mm(xf[1], wf[1], acc);
#pragma unroll
        for (int m = 0; m < 4; ++m)
#pragma unroll
            for (int n = 0; n < 5; ++n) *(LAS f32x4*)(lds + w * 20480 + (m * 5 + n) * 1024 + lo) = acc[m][n] * gv[m] + accb[m][n];
    }
    LBAR();
    asm volatile(".Lgm_k2end:");
#pragma unroll
    for (int i = 0; i < 3; ++i) {
        const int t = w + 8 * i;
        if (t < 20) {
            ldsp p = lds + t * 1024 + lo;
            const f32x4 y = ((*(const LAS f32x4*)(p) + *(const LAS f32x4*)(p + 20480)) + (*(const LAS f32x4*)(p + 40960) + *(const LAS f32x4*)(p + 61440)))
                          + ((*(const LAS f32x4*)(p + 81920) + *(const LAS f32x4*)(p + 102400)) + (*(const LAS f32x4*)(p + 122880) + *(const LAS f32x4*)(p + 143360)));
            const int m = t / 5, n = t - 5 * m;
            *(u32x2*)((char*)out + (size_t)(r0 + 32 * (m & 1) + 2 * l15 + (m >> 1)) * 1280 + ct * 160 + n * 32 + g * 8) = u32x2{pk2(y[0], y[1]), pk2(y[2], y[3])};
        }
    }
}

extern "C" void kernel_launch(void* const* d_in, const int* in_sizes, int n_in,
                              void* d_out, int out_size, void* d_ws, size_t ws_size,
                              hipStream_t stream) {
    const __hip_bfloat16* x    = (const __hip_bfloat16*)d_in[0];
    const __hip_bfloat16* wA   = (const __hip_bfloat16*)d_in[1];
    const __hip_bfloat16* wphi = (const __hip_bfloat16*)d_in[2];
    const __hip_bfloat16* woA  = (const __hip_bfloat16*)d_in[3];
    const __hip_bfloat16* wB   = (const __hip_bfloat16*)d_in[5];
    const __hip_bfloat16* woB  = (const __hip_bfloat16*)d_in[6];
    const __hip_bfloat16* gw   = (const __hip_bfloat16*)d_in[7];
    char* ws = (char*)d_ws;
    k1_qkv<<<256, 512, 0, stream>>>(x, wA, wB, wphi, gw, woA, woB, ws);
    k2_out<<<256, 512, 0, stream>>>(ws, (__hip_bfloat16*)d_out);
}
```
